```python
import torch

def _softmax(x, axis=-1):
    return torch.softmax(x, dim=axis)
def _gelu(x):
    return torch.nn.functional.gelu(x, approximate="tanh")
def _silu(x):
    return torch.nn.functional.silu(x)
def _sigmoid(x):
    return torch.sigmoid(x)
def _layernorm(x, axis=-1, eps=1e-5):
    m = x.mean(dim=axis, keepdim=True)
    v = x.var(dim=axis, keepdim=True, unbiased=False)
    return (x - m) / torch.sqrt(v + eps)
def _scatter_add(tgt, idx, src, axis=0):
    return tgt.clone().index_add_(axis, idx, src)
def _segment_sum(src, seg, n):
    out = torch.zeros((n,) + src.shape[1:], dtype=src.dtype)
    return out.index_add_(0, seg, src)
def _pad(x, pw):
    flat = tuple(v for pair in reversed(pw) for v in pair)
    return torch.nn.functional.pad(x, flat)

kernel_name = 'recurrent_scan__sliding_block_cached__glu_gated_bilinear__decode'

def setup_inputs():

    _w = (lambda a: (lambda t: t.to(torch.bfloat16) if t.is_floating_point() else t)(torch.as_tensor(a)))
    def _A_setup():
        B, Sq, D = 1, 1, 640
        Hq, Hkv, Sk, Dh = 32, 32, 8192, 192
        assert Hq % Hkv == 0, (Hq, Hkv)
        return {
            "x": _w((lambda *s: torch.randn(*s, dtype=torch.bfloat16) * 0.2)(B, Sq, D)),
            "k_cache": _w((lambda *s: torch.randn(*s, dtype=torch.bfloat16) * 1.6)(B, Hkv, Sk, Dh)),
            "v_cache": _w((lambda *s: torch.randn(*s, dtype=torch.bfloat16) * 1.6)(B, Hkv, Sk, Dh)),
            "wq": _w((lambda *s: torch.randn(*s, dtype=torch.bfloat16) * 1.6)(D, Hq * Dh) * (D ** -0.5)),
            "wo": _w((lambda *s: torch.randn(*s, dtype=torch.bfloat16) * 1.6)(Hq * Dh, D) * ((Hq * Dh) ** -0.5)),
        }
    _A_in = _A_setup()

    def _B_setup():
        B, S, D, F = 1, 1, 640, 1536
        return {
            "x": _w((lambda *s: torch.randn(*s, dtype=torch.bfloat16) * 0.2)(B, S, D)),
            "w_gate": _w((lambda *s: torch.randn(*s, dtype=torch.bfloat16) * 0.8)(D, F) * (D ** -0.5)),
            "w_up": _w((lambda *s: torch.randn(*s, dtype=torch.bfloat16) * 0.8)(D, F) * (D ** -0.5)),
            "w_down": _w((lambda *s: torch.randn(*s, dtype=torch.bfloat16) * 0.8)(F, D) * (F ** -0.5)),
        }
    _B_in = _B_setup()

    return {
        "A_x": _A_in["x"],
        "A_k_cache": _A_in["k_cache"],
        "A_v_cache": _A_in["v_cache"],
        "A_wq": _A_in["wq"],
        "A_wo": _A_in["wo"],
        "B_x": _B_in["x"],
        "B_w_gate": _B_in["w_gate"],
        "B_w_up": _B_in["w_up"],
        "B_w_down": _B_in["w_down"],
    }

def reference(A_x, A_k_cache, A_v_cache, A_wq, A_wo, B_x, B_w_gate, B_w_up, B_w_down):
    def _A_ref(x, k_cache, v_cache, wq, wo):
        B, Sq, D = x.shape
        Hq, Dh = 32, 192
        Hkv, Sk = k_cache.shape[1], k_cache.shape[2]
        W = min(4096, max(256, Sk // 4))
        rep = Hq // Hkv
        q = (lambda x, *d: x.permute(*d))((x @ wq).reshape(B, Sq, Hq, Dh), 0, 2, 1, 3)
        k = (lambda x, n, axis: torch.repeat_interleave(x, n, dim=axis))(k_cache[:, :, Sk - W:, :], rep, axis=1)
        v = (lambda x, n, axis: torch.repeat_interleave(x, n, dim=axis))(v_cache[:, :, Sk - W:, :], rep, axis=1)
        scores = torch.einsum("bhqd,bhkd->bhqk", q, k) * (Dh ** -0.5)
        p = _softmax(scores, axis=-1)
        o = torch.einsum("bhqk,bhkd->bhqd", p, v)
        return (lambda x, *d: x.permute(*d))(o, 0, 2, 1, 3).reshape(B, Sq, Hq * Dh) @ wo

    def _B_ref(x, w_gate, w_up, w_down):
        g = torch.einsum("bsd,df->bsf", x, w_gate)
        u = torch.einsum("bsd,df->bsf", x, w_up)
        h = g * u
        return torch.einsum("bsf,fd->bsd", h, w_down)

    x = A_x
    xn = _layernorm(x, axis=-1)
    s = _A_ref(xn, A_k_cache, A_v_cache, A_wq, A_wo)
    g = _silu(_B_ref(xn, B_w_gate, B_w_up, B_w_down))
    return x + s * g
```

```cpp
#include <hip/hip_runtime.h>
#include <hip/hip_bf16.h>
#include <stdint.h>

typedef unsigned short u16;
typedef unsigned u32x4 __attribute__((ext_vector_type(4)));
constexpr int D = 640, HQ = 32, SK = 8192, DH = 192, W = 2048, F = 1536, NQ = HQ * DH;

__device__ __forceinline__ float bf2f(unsigned b) { return __uint_as_float(b << 16); }
__device__ __forceinline__ float rbf(float f) { return __bfloat162float(__float2bfloat16(f)); }

__device__ void layernorm_lds(const u16* x, float* xn, float* red) {
  int tid = threadIdx.x;
  float s = 0.f;
  for (int i = tid; i < D; i += blockDim.x) s += bf2f(x[i]);
  for (int o = 32; o; o >>= 1) s += __shfl_xor(s, o);
  if ((tid & 63) == 0) red[tid >> 6] = s;
  __syncthreads();
  float tot = 0.f;
  for (int w = 0; w < blockDim.x / 64; ++w) tot += red[w];
  float mean = tot / D;
  __syncthreads();
  float v = 0.f;
  for (int i = tid; i < D; i += blockDim.x) { float d = bf2f(x[i]) - mean; v += d * d; }
  for (int o = 32; o; o >>= 1) v += __shfl_xor(v, o);
  if ((tid & 63) == 0) red[tid >> 6] = v;
  __syncthreads();
  float vt = 0.f;
  for (int w = 0; w < blockDim.x / 64; ++w) vt += red[w];
  float var = vt / D;
  float mb = rbf(mean), vb = rbf(var);
  float den = rbf(sqrtf(rbf(vb + 1e-5f)));
  for (int i = tid; i < D; i += blockDim.x) xn[i] = rbf(rbf(bf2f(x[i]) - mb) / den);
  __syncthreads();
}

__global__ void __launch_bounds__(256) k1(const u16* x, const u16* wq, const u16* wg, const u16* wu, float* q, float* g, float* u) {
  __shared__ float xn[D]; __shared__ float red[4]; __shared__ float part[32][64];
  layernorm_lds(x, xn, red);
  int b = blockIdx.x; const u16* w; int ld, c0; float* out;
  if (b < 96) { w = wq; ld = NQ; c0 = b * 64; out = q; }
  else if (b < 120) { w = wg; ld = F; c0 = (b - 96) * 64; out = g; }
  else { w = wu; ld = F; c0 = (b - 120) * 64; out = u; }
  int cg = threadIdx.x & 7, rg = threadIdx.x >> 3;
  float acc[8] = {0};
  for (int r = 0; r < 20; ++r) {
    int row = rg * 20 + r;
    u32x4 v = *(const u32x4*)(w + (size_t)row * ld + c0 + cg * 8);
    float xv = xn[row];
    for (int k = 0; k < 4; ++k) { acc[2 * k] += xv * __uint_as_float(v[k] << 16); acc[2 * k + 1] += xv * __uint_as_float(v[k] & 0xffff0000u); }
  }
  for (int k = 0; k < 8; ++k) part[rg][cg * 8 + k] = acc[k];
  __syncthreads();
  if (threadIdx.x < 64) { float s = 0; for (int r = 0; r < 32; ++r) s += part[r][threadIdx.x]; out[c0 + threadIdx.x] = rbf(s); }
}

__global__ void __launch_bounds__(256) k2(const u16* kc, const u16* vc, const float* q, float* po, float* pml) {
  __shared__ float qs[DH]; __shared__ float sc[256]; __shared__ float red[4]; __shared__ float opart[10][DH];
  int h = blockIdx.x >> 3, s = blockIdx.x & 7, tid = threadIdx.x;
  if (tid < DH) qs[tid] = q[h * DH + tid];
  __syncthreads();
  size_t base = ((size_t)h * SK + (SK - W) + s * 256) * DH;
  const u16* kr = kc + base + (size_t)tid * DH;
  float a = 0.f;
  for (int j = 0; j < 24; ++j) { u32x4 v = *(const u32x4*)(kr + j * 8);
    for (int k = 0; k < 4; ++k) { a += qs[j * 8 + 2 * k] * __uint_as_float(v[k] << 16); a += qs[j * 8 + 2 * k + 1] * __uint_as_float(v[k] & 0xffff0000u); } }
  a = rbf(rbf(a) * rbf(0.07216878364870322f));
  float m = a; for (int o = 32; o; o >>= 1) m = fmaxf(m, __shfl_xor(m, o));
  if ((tid & 63) == 0) red[tid >> 6] = m;
  __syncthreads();
  m = fmaxf(fmaxf(red[0], red[1]), fmaxf(red[2], red[3]));
  __syncthreads();
  float p = __expf(a - m); sc[tid] = p;
  float l = p; for (int o = 32; o; o >>= 1) l += __shfl_xor(l, o);
  if ((tid & 63) == 0) red[tid >> 6] = l;
  __syncthreads();
  l = red[0] + red[1] + red[2] + red[3];
  if (tid < 240) {
    int cg = tid % 24, rg = tid / 24; float acc[8] = {0};
    for (int r = rg; r < 256; r += 10) { u32x4 v = *(const u32x4*)(vc + base + (size_t)r * DH + cg * 8); float pv = sc[r];
      for (int k = 0; k < 4; ++k) { acc[2 * k] += pv * __uint_as_float(v[k] << 16); acc[2 * k + 1] += pv * __uint_as_float(v[k] & 0xffff0000u); } }
    for (int k = 0; k < 8; ++k) opart[rg][cg * 8 + k] = acc[k];
  }
  __syncthreads();
  if (tid < DH) { float t = 0; for (int r = 0; r < 10; ++r) t += opart[r][tid]; po[blockIdx.x * DH + tid] = t; }
  if (tid == 0) { pml[blockIdx.x * 2] = m; pml[blockIdx.x * 2 + 1] = l; }
}

__global__ void __launch_bounds__(256) k3(const u16* wo, const u16* wd, const float* po, const float* pml, const float* g, const float* u, float* ps) {
  __shared__ float in[24]; __shared__ float part[3][D];
  int b = blockIdx.x, tid = threadIdx.x; const u16* w; int r0;
  if (b < 256) {
    int h = b >> 3, s = b & 7; r0 = b * 24; w = wo;
    if (tid < 24) { float M = -INFINITY; for (int j = 0; j < 8; ++j) M = fmaxf(M, pml[(h * 8 + j) * 2]);
      float L = 0, o = 0; for (int j = 0; j < 8; ++j) { float e = __expf(pml[(h * 8 + j) * 2] - M); L += e * pml[(h * 8 + j) * 2 + 1]; o += e * po[(h * 8 + j) * DH + s * 24 + tid]; }
      in[tid] = rbf(o / L); }
  } else { r0 = (b - 256) * 24; w = wd; if (tid < 24) in[tid] = rbf(g[r0 + tid] * u[r0 + tid]); }
  __syncthreads();
  if (tid < 240) { int cg = tid % 80, rg = tid / 80; float acc[8] = {0};
    for (int r = rg * 8; r < rg * 8 + 8; ++r) { u32x4 v = *(const u32x4*)(w + (size_t)(r0 + r) * D + cg * 8); float pv = in[r];
      for (int k = 0; k < 4; ++k) { acc[2 * k] += pv * __uint_as_float(v[k] << 16); acc[2 * k + 1] += pv * __uint_as_float(v[k] & 0xffff0000u); } }
    for (int k = 0; k < 8; ++k) part[rg][cg * 8 + k] = acc[k]; }
  __syncthreads();
  for (int c = tid; c < D; c += 256) ps[(size_t)b * D + c] = part[0][c] + part[1][c] + part[2][c];
}

__global__ void __launch_bounds__(256) k4(const u16* x, const float* ps, u16* out) {
  __shared__ float pa[16][16], pb[16][16];
  int c = blockIdx.x * 16 + (threadIdx.x & 15), gq = threadIdx.x >> 4;
  float s = 0, d = 0;
  for (int j = gq; j < 256; j += 16) s += ps[(size_t)j * D + c];
  for (int j = 256 + gq; j < 320; j += 16) d += ps[(size_t)j * D + c];
  pa[gq][threadIdx.x & 15] = s; pb[gq][threadIdx.x & 15] = d;
  __syncthreads();
  if (threadIdx.x < 16) { float S = 0, G = 0; for (int j = 0; j < 16; ++j) { S += pa[j][threadIdx.x]; G += pb[j][threadIdx.x]; }
    S = rbf(S); G = rbf(G); float sg = rbf(G / (1.f + __expf(-G)));
    float r = bf2f(x[c]) + rbf(S * sg);
    __hip_bfloat16 o = __float2bfloat16(r); out[c] = *(u16*)&o; }
}

extern "C" void kernel_launch(void* const* d_in, const int* in_sizes, int n_in, void* d_out, int out_size, void* d_ws, size_t ws_size, hipStream_t stream) {
  const u16* x = (const u16*)d_in[0]; const u16* kc = (const u16*)d_in[1]; const u16* vc = (const u16*)d_in[2];
  const u16* wq = (const u16*)d_in[3]; const u16* wo = (const u16*)d_in[4];
  const u16* wg = (const u16*)d_in[6]; const u16* wu = (const u16*)d_in[7]; const u16* wd = (const u16*)d_in[8];
  float* ws = (float*)d_ws;
  float* q = ws; float* g = q + NQ; float* u = g + F; float* po = u + F; float* pml = po + 256 * DH; float* ps = pml + 512;
  k1<<<144, 256, 0, stream>>>(x, wq, wg, wu, q, g, u);
  k2<<<256, 256, 0, stream>>>(kc, vc, q, po, pml);
  k3<<<320, 256, 0, stream>>>(wo, wd, po, pml, g, u, ps);
  k4<<<40, 256, 0, stream>>>(x, ps, (u16*)d_out);
}
```

```cpp
#include <hip/hip_runtime.h>
#include <stdint.h>

typedef unsigned u32;
typedef unsigned long long u64;
typedef unsigned short u16;
typedef u32 u32x4 __attribute__((ext_vector_type(4)));
typedef u32 u32x2 __attribute__((ext_vector_type(2)));
typedef float f32x2 __attribute__((ext_vector_type(2)));
typedef float f32x4 __attribute__((ext_vector_type(4)));
typedef __bf16 bf16x2 __attribute__((ext_vector_type(2)));
typedef __amdgpu_buffer_rsrc_t rsrc_t;
#define LAS __attribute__((address_space(3)))

#define STREAM_AUX 2
constexpr int AUX_NT = STREAM_AUX, AUX_SC1 = 16;
constexpr int D = 640, NQ = 6144, F = 1536, SK = 8192, WIN = 2048;
constexpr int NWAVE = 15, NW_W = 5;
constexpr u32 TAG_X = 0x9E3779B9u;
constexpr int SPIN_CAP = 1 << 17;

constexpr int WS_E = 0;
constexpr int WS_ML = 4096;
constexpr int WS_QP = 8192;
constexpr int WS_GU = WS_QP + 32 * 8 * 1536;
constexpr int WS_OP = WS_GU + 2 * 8 * 12288;
constexpr int WS_DP = WS_OP + 32 * 8 * 1536;
constexpr int WS_SP = WS_DP + 32 * 5120;
constexpr int WS_END = WS_SP + 128 * 5120;
#define STAMP(i) do {} while (0)
#define STAMP_DECL() do {} while (0)
#define STAMP_FLUSH() do {} while (0)

#define QDELAY 32
#define MLDELAY 24
constexpr int L_QCNT = 0, L_TAGF = 1, L_KMC = 2, L_PCNT = 3, L_VISS = 4, L_VREC = 5  , L_OPUB = 8, L_HRDY = 9, L_ORDY = 10, L_SDONE = 11;
constexpr int L_TAG = 16, L_KM = 20, L_KL = 24;
constexpr int L_Q = 32;
constexpr int L_P = 128;
constexpr int L_REC = 256;
constexpr int L_H = 448;
constexpr int L_O = 480;
constexpr int L_TOTAL = 512;

__device__ __forceinline__ float asf(u32 x) { return __builtin_bit_cast(float, x); }
__device__ __forceinline__ u32 asu(float x) { return __builtin_bit_cast(u32, x); }
__device__ __forceinline__ u32 pack2(float lo, float hi) { bf16x2 v = {(__bf16)lo, (__bf16)hi}; return __builtin_bit_cast(u32, v); }
__device__ __forceinline__ float rbf(float f) { return (float)(__bf16)f; }
__device__ __forceinline__ float dot2(u32 a, u32 b, float acc) {
  return __builtin_amdgcn_fdot2_f32_bf16(__builtin_bit_cast(bf16x2, a), __builtin_bit_cast(bf16x2, b), acc, false);
}
template <int CTRL> __device__ __forceinline__ float dpp(float x) {
  return __builtin_bit_cast(float, __builtin_amdgcn_mov_dpp(__builtin_bit_cast(int, x), CTRL, 0xf, 0xf, true));
}
constexpr int XOR1 = 0xB1, XOR2 = 0x4E, HMIR = 0x141, ROR8 = 0x128, ROR4 = 0x124;
__device__ __forceinline__ float rs32(float a, float b) { auto r = __builtin_amdgcn_permlane32_swap(asu(a), asu(b), false, false); return asf(r[0]) + asf(r[1]); }
__device__ __forceinline__ float rs16(float a, float b) { auto r = __builtin_amdgcn_permlane16_swap(asu(a), asu(b), false, false); return asf(r[0]) + asf(r[1]); }
__device__ __forceinline__ float max32(float a) { auto r = __builtin_amdgcn_permlane32_swap(asu(a), asu(a), false, false); return fmaxf(asf(r[0]), asf(r[1])); }
__device__ __forceinline__ float max16(float a) { auto r = __builtin_amdgcn_permlane16_swap(asu(a), asu(a), false, false); return fmaxf(asf(r[0]), asf(r[1])); }
__device__ __forceinline__ float sum_hi(float a) { a += dpp<ROR8>(a); a = rs16(a, a); return rs32(a, a); }
__device__ __forceinline__ float max_hi(float a) { a = fmaxf(a, dpp<ROR8>(a)); return max32(max16(a)); }
__device__ __forceinline__ float sum_lo(float a) { a += dpp<XOR1>(a); a += dpp<XOR2>(a); a += dpp<HMIR>(a); return a; }
__device__ __forceinline__ float max_lo(float a) { a = fmaxf(a, dpp<XOR1>(a)); a = fmaxf(a, dpp<XOR2>(a)); return fmaxf(a, dpp<HMIR>(a)); }
__device__ __forceinline__ u32 plo(u32 a, u32 b) { return __builtin_amdgcn_perm(b, a, 0x05040100u); }
__device__ __forceinline__ u32 phi(u32 a, u32 b) { return __builtin_amdgcn_perm(b, a, 0x07060302u); }
__device__ __forceinline__ rsrc_t mk(const void* p, u32 bytes) { return __builtin_amdgcn_make_buffer_rsrc((void*)p, 0, bytes, 0x00020000); }
__device__ __forceinline__ u32x4 ldnt(rsrc_t r, int voff, int soff) { return __builtin_amdgcn_raw_buffer_load_b128(r, voff, soff, AUX_NT); }
__device__ __forceinline__ u32x4 ldsc(rsrc_t r, int voff, int soff) { return __builtin_amdgcn_raw_buffer_load_b128(r, voff, soff, AUX_SC1); }
__device__ __forceinline__ void stsc(rsrc_t r, int voff, float v0, float v1, u32 tag) {
  u32x4 v = {asu(v0), tag, asu(v1), tag ^ TAG_X};
  __builtin_amdgcn_raw_buffer_store_b128(v, r, voff, 0, AUX_SC1);
}
__device__ __forceinline__ u32 bad(u32 acc, u32x4 v, u32 tag) { return acc | (v[1] ^ tag) | (v[3] ^ (tag ^ TAG_X)); }
__device__ __forceinline__ bool all_zero(u32 x) { return __builtin_amdgcn_ballot_w64(x != 0u) == 0ull; }
#define CFENCE() asm volatile("" ::: "memory")
#define SB() __builtin_amdgcn_sched_barrier(0)
#define LGKM0() asm volatile("s_waitcnt lgkmcnt(0)" ::: "memory")
#define PIN(x) asm volatile("" : "+v"(x))

__device__ __forceinline__ void lds_wait_ge(volatile LAS u32* p, u32 want) {
#pragma nounroll
  for (int i = 0; i < SPIN_CAP * 8; ++i) { if (__builtin_amdgcn_readfirstlane(*p) >= want) break; __builtin_amdgcn_s_sleep(1); }
  CFENCE();
}
__device__ __forceinline__ void lds_inc(LAS u32* p, int lane) {
  LGKM0();
  if (lane == 0) asm volatile("ds_add_u32 %0, %1" :: "v"((u32)(uintptr_t)p), "v"(1u) : "memory");
}
__device__ __forceinline__ u32 lds_inc_rtn(LAS u32* p, int lane) {
  LGKM0();
  u32 old = 0;
  if (lane == 0) asm volatile("ds_add_rtn_u32 %0, %1, %2\n\ts_waitcnt lgkmcnt(0)" : "=v"(old) : "v"((u32)(uintptr_t)p), "v"(1u) : "memory");
  return __builtin_amdgcn_readfirstlane(old);
}
__device__ __forceinline__ void red8(float (&acc)[8], float& o0, float& o1) {
  float a0 = rs32(acc[0], acc[4]), a1 = rs32(acc[1], acc[5]), a2 = rs32(acc[2], acc[6]), a3 = rs32(acc[3], acc[7]);
  float b0 = rs16(a0, a2), b1 = rs16(a1, a3);
  o0 = b0 + dpp<ROR8>(b0); o1 = b1 + dpp<ROR8>(b1);
}
__device__ __forceinline__ void mac2(float (&acc)[8], u32x4 ra, u32x4 rb, u32 in) {
#pragma unroll
  for (int k = 0; k < 4; ++k) { acc[2 * k] = dot2(plo(ra[k], rb[k]), in, acc[2 * k]); acc[2 * k + 1] = dot2(phi(ra[k], rb[k]), in, acc[2 * k + 1]); }
}

__global__ void __launch_bounds__(NWAVE * 64, 4)
fused_kernel(const u16* __restrict__ x, const u16* __restrict__ kc, const u16* __restrict__ vc, const u16* __restrict__ wq, const u16* __restrict__ wo,
             const u16* __restrict__ wg, const u16* __restrict__ wu, const u16* __restrict__ wd, u16* __restrict__ out, void* ws) {
  __shared__ __attribute__((aligned(16))) u32 lds_raw[L_TOTAL];
  LAS u32* lds = (LAS u32*)lds_raw;
  volatile LAS u32* vl = (volatile LAS u32*)lds_raw;
  const int b = blockIdx.x, tid = threadIdx.x, lane = tid & 63, wave = __builtin_amdgcn_readfirstlane(tid >> 6);
  const int h = b & 31, s = b >> 5, r = lane >> 3, c = lane & 7;
  const int pos = (lane >> 4) * 8 + c;
  const rsrc_t rws = mk(ws, WS_END);
  STAMP_DECL();
  STAMP(0);
  if (wave < NW_W) {
    const int w = wave;
    __builtin_amdgcn_s_setprio(3);
    const bool isd = b < 64;
    const int idx = b - 64, cb = idx % 24, rc = idx / 24;
    const bool p1 = (w < 3) || !isd;
    if (w == 0) { if (lane < 16) lds[lane] = 0; LGKM0(); }
    if (w >= 3) __builtin_amdgcn_s_barrier();
    u32 tag;
    if (p1) {
      const u32 e0 = __builtin_amdgcn_raw_buffer_load_b32(rws, WS_E, 0, AUX_SC1);
      const rsrc_t rx = mk(x, D * 2);
      const int rb = (w < 3) ? 80 * s : 80 * rc;
      const u32x4 xs4 = __builtin_amdgcn_raw_buffer_load_b128(rx, 20 * lane, 0, 0); const u32 xs1 = __builtin_amdgcn_raw_buffer_load_b32(rx, 20 * lane + 16, 0, 0);
      const u32x4 xo4 = __builtin_amdgcn_raw_buffer_load_b128(rx, 2 * rb + 20 * r, 0, 0); const u32 xo1 = __builtin_amdgcn_raw_buffer_load_b32(rx, 2 * rb + 20 * r + 16, 0, 0);
      const int ld2 = (w < 3) ? NQ * 2 : F * 2;
      const rsrc_t rw = mk(w < 3 ? wq : (w == 3 ? wg : wu), D * ld2);
      const int voff = (rb + 10 * r) * ld2 + ((w < 3) ? (h * 192 + 64 * w) * 2 : cb * 128) + 16 * c;
      u32x4 a[10];
#pragma unroll
      for (int i = 0; i < 10; ++i) a[i] = ldnt(rw, voff, i * ld2);
      SB();
      if (w < 3) __builtin_amdgcn_s_barrier();
      STAMP(1);
      tag = __builtin_amdgcn_readfirstlane(e0) + 1u;
      float xv[10];
#pragma unroll
      for (int k = 0; k < 4; ++k) { xv[2 * k] = asf(xs4[k] << 16); xv[2 * k + 1] = asf(xs4[k] & 0xffff0000u); }
      xv[8] = asf(xs1 << 16); xv[9] = asf(xs1 & 0xffff0000u);
      float sm = 0.f;
#pragma unroll
      for (int k = 0; k < 10; ++k) sm += xv[k];
      const float mean = sum_hi(sum_lo(sm)) * (1.0f / D);
      float sv = 0.f;
#pragma unroll
      for (int k = 0; k < 10; ++k) { const float dlt = xv[k] - mean; sv = fmaf(dlt, dlt, sv); }
      const float var = sum_hi(sum_lo(sv)) * (1.0f / D);
      const float mb = rbf(mean), den = rbf(sqrtf(rbf(rbf(var) + 1e-5f)));
      const float rden = 1.0f / den;
      u32 xn[5];
#pragma unroll
      for (int k = 0; k < 5; ++k) { const u32 xr = k < 4 ? xo4[k] : xo1;
        xn[k] = pack2(rbf(asf(xr << 16) - mb) * rden, rbf(asf(xr & 0xffff0000u) - mb) * rden); PIN(xn[k]); }
      SB();
      float acc[8];
#pragma unroll
      for (int k = 0; k < 8; ++k) acc[k] = 0.f;
#pragma unroll
      for (int i = 0; i < 5; ++i) { mac2(acc, a[2 * i], a[2 * i + 1], xn[i]); SB(); }
      float o0, o1; red8(acc, o0, o1);
      const int dst = (w < 3) ? WS_QP + (h * 8 + s) * 1536 + w * 512 : WS_GU + ((w - 3) * 8 + rc) * 12288 + cb * 512;
      if (!(lane & 8)) stsc(rws, dst + pos * 16, o0, o1, tag);
    } else {
      tag = __builtin_amdgcn_readfirstlane(__builtin_amdgcn_raw_buffer_load_b32(rws, WS_E, 0, AUX_SC1)) + 1u;
    }
    if (w >= 3) lds_inc(lds + L_QCNT, lane);
    STAMP(2);
    u32x4 t[6];
    if (isd) {
      const rsrc_t rd = mk(wd, F * D * 2);
      const int voff = r * 8 * 1280 + (b & 1) * 640 + 128 * w + 16 * c;
#pragma unroll
      for (int i = 0; i < 6; ++i) { const int G = 3 * (b >> 1) + (i >> 1); t[i] = ldnt(rd, voff, ((G >> 2) * 64 + (G & 3) * 2 + (i & 1)) * 1280); }
      SB();
    }
    if (w < 3) {
      __builtin_amdgcn_s_sleep(QDELAY);
      const int src = WS_QP + (h * 8 + (lane >> 5)) * 1536 + w * 512 + (lane & 31) * 16;
      u32x4 g[4];
#pragma nounroll
      for (int it = 0; it < SPIN_CAP; ++it) {
        CFENCE();
#pragma unroll
        for (int i = 0; i < 4; ++i) g[i] = ldsc(rws, src, i * 3072);
        u32 e = 0;
#pragma unroll
        for (int i = 0; i < 4; ++i) e = bad(e, g[i], tag);
        if (all_zero(e)) break;
      }
      float q0 = (asf(g[0][0]) + asf(g[1][0])) + (asf(g[2][0]) + asf(g[3][0]));
      float q1 = (asf(g[0][2]) + asf(g[1][2])) + (asf(g[2][2]) + asf(g[3][2]));
      q0 = rs32(q0, q0); q1 = rs32(q1, q1);
      const int p = lane & 31;
      if (lane < 32) lds[L_Q + 32 * w + 4 * (p & 7) + (p >> 3)] = pack2(q0, q1);
      if (lane == 0) lds[L_TAG] = tag;
      lds_inc(lds + L_QCNT, lane);
    }
    STAMP(3);
    if (isd) {
      if (w == 3) {
        lds_wait_ge(vl + L_QCNT, 5u);
        const int src = WS_GU + r * 12288 + (3 * (b >> 1) * 8 + c) * 16;
        u32x4 g[6];
#pragma nounroll
        for (int it = 0; it < SPIN_CAP; ++it) {
          CFENCE();
#pragma unroll
          for (int i = 0; i < 6; ++i) g[i] = ldsc(rws, src + (i >> 1) * 128, (i & 1) * 8 * 12288);
          u32 e = 0;
#pragma unroll
          for (int i = 0; i < 6; ++i) e = bad(e, g[i], tag);
          if (all_zero(e)) break;
        }
        float ag[8] = {asf(g[0][0]), asf(g[0][2]), asf(g[2][0]), asf(g[2][2]), asf(g[4][0]), asf(g[4][2]), 0.f, 0.f};
        float au[8] = {asf(g[1][0]), asf(g[1][2]), asf(g[3][0]), asf(g[3][2]), asf(g[5][0]), asf(g[5][2]), 0.f, 0.f};
        float g0, g1, u0, u1; red8(ag, g0, g1); red8(au, u0, u1);
        if (lane < 48) lds[L_H + pos] = pack2(rbf(g0) * rbf(u0), rbf(g1) * rbf(u1));
        lds_inc(lds + L_HRDY, lane);
      }
      lds_wait_ge(vl + L_HRDY, 1u);
      float acc[8];
#pragma unroll
      for (int k = 0; k < 8; ++k) acc[k] = 0.f;
#pragma unroll
      for (int i = 0; i < 3; ++i) mac2(acc, t[2 * i], t[2 * i + 1], lds[L_H + 8 * i + r]);
      float o0, o1; red8(acc, o0, o1);
      if (!(lane & 8)) stsc(rws, WS_DP + (b >> 1) * 5120 + (b & 1) * 2560 + w * 512 + pos * 16, o0, o1, tag);
    }
    STAMP(4);
    lds_wait_ge(vl + L_VISS, 6u);
    {
      const rsrc_t ro = mk(wo, NQ * D * 2);
      const int voff = (h * 192 + r * 8) * 1280 + (s & 1) * 640 + 128 * w + 16 * c;
#pragma unroll
      for (int i = 0; i < 6; ++i) { const int G = 3 * (s >> 1) + (i >> 1); t[i] = ldnt(ro, voff, ((G >> 2) * 64 + (G & 3) * 2 + (i & 1)) * 1280); }
    }
    SB();
    STAMP(5);
    u32 pr[3][8];
#pragma unroll
    for (int i = 0; i < 3; ++i)
#pragma unroll
      for (int k = 0; k < 4; ++k) { pr[i][2 * k] = plo(t[2 * i][k], t[2 * i + 1][k]); pr[i][2 * k + 1] = phi(t[2 * i][k], t[2 * i + 1][k]); PIN(pr[i][2 * k]); PIN(pr[i][2 * k + 1]); }
    STAMP(6);
    lds_wait_ge(vl + L_ORDY, 1u);
    STAMP(7);
    {
      float acc[8];
#pragma unroll
      for (int k = 0; k < 8; ++k) acc[k] = 0.f;
#pragma unroll
      for (int i = 0; i < 3; ++i) { const u32 op = lds[L_O + 8 * i + r];
#pragma unroll
        for (int k = 0; k < 8; ++k) acc[k] = dot2(pr[i][k], op, acc[k]); }
      float o0, o1; red8(acc, o0, o1);
      if (!(lane & 8)) stsc(rws, WS_SP + (h * 4 + (s >> 1)) * 5120 + (s & 1) * 2560 + w * 512 + pos * 16, o0, o1, tag);
      if (lane == 0) vl[L_SDONE] = 1u;
      STAMP(8);
    }
  } else if (wave < 9) {
    const int kw = wave - 5;
    __builtin_amdgcn_s_setprio(2);
    const rsrc_t rk = mk(kc, 32u * SK * 384u);
    const int voff = (h * SK + (SK - WIN) + 256 * s + 64 * kw + r) * 384 + 16 * c;
    __builtin_amdgcn_s_barrier();
    u32x4 K[24];
#pragma unroll
    for (int i = 0; i < 24; ++i) { K[i] = ldnt(rk, voff + (i % 3) * 128, (i / 3) * 3072); if (i % 3 == 2) SB(); }
    STAMP(1);
    lds_wait_ge(vl + L_QCNT, 5u);
    STAMP(2);
    const u32 tag = __builtin_amdgcn_readfirstlane(vl[L_TAG]);
    u32x4 q[3];
#pragma unroll
    for (int t = 0; t < 3; ++t) q[t] = *(LAS u32x4*)(lds + L_Q + 32 * t + 4 * c);
    float sc[8];
#pragma unroll
    for (int g = 0; g < 8; ++g) {
      float d0 = 0.f;
#pragma unroll
      for (int t = 0; t < 3; ++t) { d0 = dot2(K[3 * g + t][0], q[t][0], d0); d0 = dot2(K[3 * g + t][1], q[t][1], d0); d0 = dot2(K[3 * g + t][2], q[t][2], d0); d0 = dot2(K[3 * g + t][3], q[t][3], d0); }
      sc[g] = d0; SB();
    }
#pragma unroll
    for (int g = 0; g < 8; ++g) sc[g] = rbf(rbf(sum_lo(sc[g])) * 0.07216878364870322f) * 1.4426950408889634f;
    const float mw = max_hi(fmaxf(fmaxf(fmaxf(sc[0], sc[1]), fmaxf(sc[2], sc[3])), fmaxf(fmaxf(sc[4], sc[5]), fmaxf(sc[6], sc[7]))));
    if (lane == 0) lds[L_KM + kw] = asu(mw);
    lds_inc(lds + L_KMC, lane);
    STAMP(3);
    lds_wait_ge(vl + L_KMC, 4u);
    const f32x4 m4 = *(LAS f32x4*)(lds + L_KM);
    const float M = fmaxf(fmaxf(m4[0], m4[1]), fmaxf(m4[2], m4[3]));
    float p[8];
#pragma unroll
    for (int g = 0; g < 8; ++g) p[g] = __builtin_amdgcn_exp2f(sc[g] - M);
    if (c == 0) *(LAS u32x4*)(lds + L_P + (kw >> 1) * 64 + r * 8 + (kw & 1) * 4) = u32x4{pack2(p[0], p[1]), pack2(p[2], p[3]), pack2(p[4], p[5]), pack2(p[6], p[7])};
    const float lw = sum_hi(((p[0] + p[1]) + (p[2] + p[3])) + ((p[4] + p[5]) + (p[6] + p[7])));
    if (lane == 0) lds[L_KL + kw] = asu(lw);
    if (lds_inc_rtn(lds + L_PCNT, lane) == 3u) {
      const f32x4 l4 = *(LAS f32x4*)(lds + L_KL);
      if (lane == 0) stsc(rws, WS_ML + (h * 8 + s) * 16, M, (l4[0] + l4[1]) + (l4[2] + l4[3]), tag);
    }
    STAMP(4);
    if (kw == 0) {
      __builtin_amdgcn_s_setprio(3);
      __builtin_amdgcn_s_sleep(MLDELAY);
      u32x4 ml;
#pragma nounroll
      for (int it = 0; it < SPIN_CAP; ++it) {
        CFENCE();
        ml = ldsc(rws, WS_ML + (h * 8 + r) * 16, 0);
        if (all_zero(bad(0u, ml, tag))) break;
      }
      const float m = asf(ml[0]), MM = max_hi(m);
      const float wj = __builtin_amdgcn_exp2f(m - MM);
      float wn = wj / sum_hi(wj * asf(ml[2]));
      PIN(wn);
      STAMP(5);
      lds_wait_ge(vl + L_OPUB, 3u);
      STAMP(6);
      const int src = WS_OP + (h * 8 + r) * 1536 + (24 * (s >> 1) + c) * 16;
      u32x4 g[3];
#pragma nounroll
      for (int it = 0; it < SPIN_CAP; ++it) {
        CFENCE();
#pragma unroll
        for (int i = 0; i < 3; ++i) g[i] = ldsc(rws, src + i * 128, 0);
        if (all_zero(bad(bad(bad(0u, g[0], tag), g[1], tag), g[2], tag))) break;
      }
      STAMP(7);
      float ao[8] = {wn * asf(g[0][0]), wn * asf(g[0][2]), wn * asf(g[1][0]), wn * asf(g[1][2]), wn * asf(g[2][0]), wn * asf(g[2][2]), 0.f, 0.f};
      float o0, o1; red8(ao, o0, o1);
      if (lane < 48) lds[L_O + pos] = pack2(o0, o1);
      lds_inc(lds + L_ORDY, lane);
      STAMP(8);
    } else if (kw == 1 && b >= 96) {
      const int tk = b - 96;
      const int ps = 2 * tk + (lane & 1), pw = ps % 160, p5 = pw & 31;
      const int col = (ps / 160) * 320 + (pw >> 5) * 64 + 8 * (p5 & 7) + 2 * (p5 >> 3) + (lane >> 5);
      const int so = (lane >> 1) * 5120 + ps * 16;
      lds_wait_ge(vl + L_VISS, 6u);
      float xv = asf((u32)x[col] << 16);
      u32x4 dg;
#pragma nounroll
      for (int it = 0; it < SPIN_CAP; ++it) {
        CFENCE();
        dg = ldsc(rws, WS_DP + so, 0);
        if (all_zero(bad(0u, dg, tag))) break;
      }
      float dv = rs32(asf(dg[0]), asf(dg[2]));
      dv = rs16(dv, dv); dv += dpp<ROR8>(dv); dv += dpp<ROR4>(dv); dv += dpp<XOR2>(dv);
      dv = rbf(dv);
      float gate = rbf(dv / (1.0f + __expf(-dv)));
      PIN(gate); PIN(xv);
      STAMP(5);
      lds_wait_ge(vl + L_SDONE, 1u);
      STAMP(6);
      u32x4 g[4];
#pragma nounroll
      for (int it = 0; it < SPIN_CAP; ++it) {
        CFENCE();
#pragma unroll
        for (int i = 0; i < 4; ++i) g[i] = ldsc(rws, WS_SP + so, i * 32 * 5120);
        u32 e = 0;
#pragma unroll
        for (int i = 0; i < 4; ++i) e = bad(e, g[i], tag);
        if (all_zero(e)) break;
      }
      STAMP(7);
      const float s0 = (asf(g[0][0]) + asf(g[1][0])) + (asf(g[2][0]) + asf(g[3][0]));
      const float s1 = (asf(g[0][2]) + asf(g[1][2])) + (asf(g[2][2]) + asf(g[3][2]));
      float sv = rs32(s0, s1);
      sv = rs16(sv, sv); sv += dpp<ROR8>(sv); sv += dpp<ROR4>(sv); sv += dpp<XOR2>(sv);
      const float res = xv + rbf(rbf(sv) * gate);
      if ((lane & 30) == 0) out[col] = __builtin_bit_cast(u16, (__bf16)res);
      if (tk == 0 && lane == 0) __builtin_amdgcn_raw_buffer_store_b32(tag, rws, WS_E, 0, AUX_SC1);
      STAMP(8);
    }
  } else {
    const int vw = wave - 9, half = vw >= 3, t = half ? vw - 3 : vw;
    __builtin_amdgcn_s_setprio(1);
    const rsrc_t rv = mk(vc, 32u * SK * 384u);
    const int voff = (h * SK + (SK - WIN) + 256 * s + 128 * half + r) * 384 + 128 * t + 16 * c;
    __builtin_amdgcn_s_barrier();
    u32x4 V[16];
#pragma unroll
    for (int i = 0; i < 16; ++i) { V[i] = ldnt(rv, voff, i * 3072); if (i & 1) SB(); }
    lds_inc(lds + L_VISS, lane);
    STAMP(1);
    lds_wait_ge(vl + L_PCNT, 4u);
    STAMP(2);
    const u32 tag = __builtin_amdgcn_readfirstlane(vl[L_TAG]);
    const u32x4 pa = *(LAS u32x4*)(lds + L_P + half * 64 + r * 8), pb = *(LAS u32x4*)(lds + L_P + half * 64 + r * 8 + 4);
    float acc[8];
#pragma unroll
    for (int k = 0; k < 8; ++k) acc[k] = 0.f;
#pragma unroll
    for (int n = 0; n < 7; ++n) { mac2(acc, V[2 * n], V[2 * n + 1], n < 4 ? pa[n] : pb[n - 4]); SB(); }
#pragma unroll
    for (int k = 0; k < 8; ++k) PIN(acc[k]);
    f32x2 rec = {0.f, 0.f};
    if (half) { lds_wait_ge(vl + L_VREC + t, 1u); rec = *(LAS f32x2*)(lds + L_REC + t * 64 + pos * 2); }
    PIN(rec);
    STAMP(3);
    mac2(acc, V[14], V[15], pb[3]);
    float o0, o1; red8(acc, o0, o1);
    if (half) {
      if (!(lane & 8)) stsc(rws, WS_OP + (h * 8 + s) * 1536 + t * 512 + pos * 16, o0 + rec[0], o1 + rec[1], tag);
      lds_inc(lds + L_OPUB, lane);
    } else {
      if (!(lane & 8)) *(LAS f32x2*)(lds + L_REC + t * 64 + pos * 2) = f32x2{o0, o1};
      lds_inc(lds + L_VREC + t, lane);
    }
    STAMP(4);
  }
  STAMP_FLUSH();
}

extern "C" void kernel_launch(void* const* d_in, const int* in_sizes, int n_in, void* d_out, int out_size, void* d_ws, size_t ws_size, hipStream_t stream) {
  fused_kernel<<<256, NWAVE * 64, 0, stream>>>((const u16*)d_in[0], (const u16*)d_in[1], (const u16*)d_in[2], (const u16*)d_in[3], (const u16*)d_in[4],
                                               (const u16*)d_in[6], (const u16*)d_in[7], (const u16*)d_in[8], (u16*)d_out, d_ws);
}
```

```cpp
#define PROBE_B 96
#define PROBE_W 6
#define PROBE_I 0
#include <hip/hip_runtime.h>
#include <stdint.h>

typedef unsigned u32;
typedef unsigned long long u64;
typedef unsigned short u16;
typedef u32 u32x4 __attribute__((ext_vector_type(4)));
typedef u32 u32x2 __attribute__((ext_vector_type(2)));
typedef float f32x2 __attribute__((ext_vector_type(2)));
typedef float f32x4 __attribute__((ext_vector_type(4)));
typedef __bf16 bf16x2 __attribute__((ext_vector_type(2)));
typedef __amdgpu_buffer_rsrc_t rsrc_t;
#define LAS __attribute__((address_space(3)))

#define STREAM_AUX 2
constexpr int AUX_NT = STREAM_AUX, AUX_SC1 = 16;
constexpr int D = 640, NQ = 6144, F = 1536, SK = 8192, WIN = 2048;
constexpr int NWAVE = 15, NW_W = 5;
constexpr u32 TAG_X = 0x9E3779B9u;
constexpr int SPIN_CAP = 1 << 17;

constexpr int WS_E = 0;
constexpr int WS_ML = 4096;
constexpr int WS_QP = 8192;
constexpr int WS_GU = WS_QP + 32 * 8 * 1536;
constexpr int WS_OP = WS_GU + 2 * 8 * 12288;
constexpr int WS_DP = WS_OP + 32 * 8 * 1536;
constexpr int WS_SP = WS_DP + 32 * 5120;
constexpr int WS_END = WS_SP + 128 * 5120;
#define STAMP(i) do { SB(); asm volatile("s_memrealtime %0\n\ts_waitcnt lgkmcnt(0)" : "=s"(stv[i]) :: "memory"); SB(); } while (0)
#define STAMP_DECL() u64 stv[12] = {0, 0, 0, 0, 0, 0, 0, 0, 0, 0, 0, 0}
#define STAMP_FLUSH() do { if (b == PROBE_B && wave == PROBE_W) { const u64 tgt_ = stv[0] + 5000 + 4 * (stv[PROBE_I] - stv[0]); \
  while (__builtin_amdgcn_s_memrealtime() < tgt_) __builtin_amdgcn_s_sleep(1); } } while (0)

#define QDELAY 32
#define MLDELAY 24
constexpr int L_QCNT = 0, L_TAGF = 1, L_KMC = 2, L_PCNT = 3, L_VISS = 4, L_VREC = 5  , L_OPUB = 8, L_HRDY = 9, L_ORDY = 10, L_SDONE = 11;
constexpr int L_TAG = 16, L_KM = 20, L_KL = 24;
constexpr int L_Q = 32;
constexpr int L_P = 128;
constexpr int L_REC = 256;
constexpr int L_H = 448;
constexpr int L_O = 480;
constexpr int L_TOTAL = 512;

__device__ __forceinline__ float asf(u32 x) { return __builtin_bit_cast(float, x); }
__device__ __forceinline__ u32 asu(float x) { return __builtin_bit_cast(u32, x); }
__device__ __forceinline__ u32 pack2(float lo, float hi) { bf16x2 v = {(__bf16)lo, (__bf16)hi}; return __builtin_bit_cast(u32, v); }
__device__ __forceinline__ float rbf(float f) { return (float)(__bf16)f; }
__device__ __forceinline__ float dot2(u32 a, u32 b, float acc) {
  return __builtin_amdgcn_fdot2_f32_bf16(__builtin_bit_cast(bf16x2, a), __builtin_bit_cast(bf16x2, b), acc, false);
}
template <int CTRL> __device__ __forceinline__ float dpp(float x) {
  return __builtin_bit_cast(float, __builtin_amdgcn_mov_dpp(__builtin_bit_cast(int, x), CTRL, 0xf, 0xf, true));
}
constexpr int XOR1 = 0xB1, XOR2 = 0x4E, HMIR = 0x141, ROR8 = 0x128, ROR4 = 0x124;
__device__ __forceinline__ float rs32(float a, float b) { auto r = __builtin_amdgcn_permlane32_swap(asu(a), asu(b), false, false); return asf(r[0]) + asf(r[1]); }
__device__ __forceinline__ float rs16(float a, float b) { auto r = __builtin_amdgcn_permlane16_swap(asu(a), asu(b), false, false); return asf(r[0]) + asf(r[1]); }
__device__ __forceinline__ float max32(float a) { auto r = __builtin_amdgcn_permlane32_swap(asu(a), asu(a), false, false); return fmaxf(asf(r[0]), asf(r[1])); }
__device__ __forceinline__ float max16(float a) { auto r = __builtin_amdgcn_permlane16_swap(asu(a), asu(a), false, false); return fmaxf(asf(r[0]), asf(r[1])); }
__device__ __forceinline__ float sum_hi(float a) { a += dpp<ROR8>(a); a = rs16(a, a); return rs32(a, a); }
__device__ __forceinline__ float max_hi(float a) { a = fmaxf(a, dpp<ROR8>(a)); return max32(max16(a)); }
__device__ __forceinline__ float sum_lo(float a) { a += dpp<XOR1>(a); a += dpp<XOR2>(a); a += dpp<HMIR>(a); return a; }
__device__ __forceinline__ float max_lo(float a) { a = fmaxf(a, dpp<XOR1>(a)); a = fmaxf(a, dpp<XOR2>(a)); return fmaxf(a, dpp<HMIR>(a)); }
__device__ __forceinline__ u32 plo(u32 a, u32 b) { return __builtin_amdgcn_perm(b, a, 0x05040100u); }
__device__ __forceinline__ u32 phi(u32 a, u32 b) { return __builtin_amdgcn_perm(b, a, 0x07060302u); }
__device__ __forceinline__ rsrc_t mk(const void* p, u32 bytes) { return __builtin_amdgcn_make_buffer_rsrc((void*)p, 0, bytes, 0x00020000); }
__device__ __forceinline__ u32x4 ldnt(rsrc_t r, int voff, int soff) { return __builtin_amdgcn_raw_buffer_load_b128(r, voff, soff, AUX_NT); }
__device__ __forceinline__ u32x4 ldsc(rsrc_t r, int voff, int soff) { return __builtin_amdgcn_raw_buffer_load_b128(r, voff, soff, AUX_SC1); }
__device__ __forceinline__ void stsc(rsrc_t r, int voff, float v0, float v1, u32 tag) {
  u32x4 v = {asu(v0), tag, asu(v1), tag ^ TAG_X};
  __builtin_amdgcn_raw_buffer_store_b128(v, r, voff, 0, AUX_SC1);
}
__device__ __forceinline__ u32 bad(u32 acc, u32x4 v, u32 tag) { return acc | (v[1] ^ tag) | (v[3] ^ (tag ^ TAG_X)); }
__device__ __forceinline__ bool all_zero(u32 x) { return __builtin_amdgcn_ballot_w64(x != 0u) == 0ull; }
#define CFENCE() asm volatile("" ::: "memory")
#define SB() __builtin_amdgcn_sched_barrier(0)
#define LGKM0() asm volatile("s_waitcnt lgkmcnt(0)" ::: "memory")
#define PIN(x) asm volatile("" : "+v"(x))

__device__ __forceinline__ void lds_wait_ge(volatile LAS u32* p, u32 want) {
#pragma nounroll
  for (int i = 0; i < SPIN_CAP * 8; ++i) { if (__builtin_amdgcn_readfirstlane(*p) >= want) break; __builtin_amdgcn_s_sleep(1); }
  CFENCE();
}
__device__ __forceinline__ void lds_inc(LAS u32* p, int lane) {
  LGKM0();
  if (lane == 0) asm volatile("ds_add_u32 %0, %1" :: "v"((u32)(uintptr_t)p), "v"(1u) : "memory");
}
__device__ __forceinline__ u32 lds_inc_rtn(LAS u32* p, int lane) {
  LGKM0();
  u32 old = 0;
  if (lane == 0) asm volatile("ds_add_rtn_u32 %0, %1, %2\n\ts_waitcnt lgkmcnt(0)" : "=v"(old) : "v"((u32)(uintptr_t)p), "v"(1u) : "memory");
  return __builtin_amdgcn_readfirstlane(old);
}
__device__ __forceinline__ void red8(float (&acc)[8], float& o0, float& o1) {
  float a0 = rs32(acc[0], acc[4]), a1 = rs32(acc[1], acc[5]), a2 = rs32(acc[2], acc[6]), a3 = rs32(acc[3], acc[7]);
  float b0 = rs16(a0, a2), b1 = rs16(a1, a3);
  o0 = b0 + dpp<ROR8>(b0); o1 = b1 + dpp<ROR8>(b1);
}
__device__ __forceinline__ void mac2(float (&acc)[8], u32x4 ra, u32x4 rb, u32 in) {
#pragma unroll
  for (int k = 0; k < 4; ++k) { acc[2 * k] = dot2(plo(ra[k], rb[k]), in, acc[2 * k]); acc[2 * k + 1] = dot2(phi(ra[k], rb[k]), in, acc[2 * k + 1]); }
}

__global__ void __launch_bounds__(NWAVE * 64, 4)
fused_kernel(const u16* __restrict__ x, const u16* __restrict__ kc, const u16* __restrict__ vc, const u16* __restrict__ wq, const u16* __restrict__ wo,
             const u16* __restrict__ wg, const u16* __restrict__ wu, const u16* __restrict__ wd, u16* __restrict__ out, void* ws) {
  __shared__ __attribute__((aligned(16))) u32 lds_raw[L_TOTAL];
  LAS u32* lds = (LAS u32*)lds_raw;
  volatile LAS u32* vl = (volatile LAS u32*)lds_raw;
  const int b = blockIdx.x, tid = threadIdx.x, lane = tid & 63, wave = __builtin_amdgcn_readfirstlane(tid >> 6);
  const int h = b & 31, s = b >> 5, r = lane >> 3, c = lane & 7;
  const int pos = (lane >> 4) * 8 + c;
  const rsrc_t rws = mk(ws, WS_END);
  STAMP_DECL();
  STAMP(0);
  if (wave < NW_W) {
    const int w = wave;
    __builtin_amdgcn_s_setprio(3);
    const bool isd = b < 64;
    const int idx = b - 64, cb = idx % 24, rc = idx / 24;
    const bool p1 = (w < 3) || !isd;
    if (w == 0) { if (lane < 16) lds[lane] = 0; LGKM0(); }
    if (w >= 3) __builtin_amdgcn_s_barrier();
    u32 tag;
    if (p1) {
      const u32 e0 = __builtin_amdgcn_raw_buffer_load_b32(rws, WS_E, 0, AUX_SC1);
      const rsrc_t rx = mk(x, D * 2);
      const int rb = (w < 3) ? 80 * s : 80 * rc;
      const u32x4 xs4 = __builtin_amdgcn_raw_buffer_load_b128(rx, 20 * lane, 0, 0); const u32 xs1 = __builtin_amdgcn_raw_buffer_load_b32(rx, 20 * lane + 16, 0, 0);
      const u32x4 xo4 = __builtin_amdgcn_raw_buffer_load_b128(rx, 2 * rb + 20 * r, 0, 0); const u32 xo1 = __builtin_amdgcn_raw_buffer_load_b32(rx, 2 * rb + 20 * r + 16, 0, 0);
      const int ld2 = (w < 3) ? NQ * 2 : F * 2;
      const rsrc_t rw = mk(w < 3 ? wq : (w == 3 ? wg : wu), D * ld2);
      const int voff = (rb + 10 * r) * ld2 + ((w < 3) ? (h * 192 + 64 * w) * 2 : cb * 128) + 16 * c;
      u32x4 a[10];
#pragma unroll
      for (int i = 0; i < 10; ++i) a[i] = ldnt(rw, voff, i * ld2);
      SB();
      if (w < 3) __builtin_amdgcn_s_barrier();
      STAMP(1);
      tag = __builtin_amdgcn_readfirstlane(e0) + 1u;
      float xv[10];
#pragma unroll
      for (int k = 0; k < 4; ++k) { xv[2 * k] = asf(xs4[k] << 16); xv[2 * k + 1] = asf(xs4[k] & 0xffff0000u); }
      xv[8] = asf(xs1 << 16); xv[9] = asf(xs1 & 0xffff0000u);
      float sm = 0.f;
#pragma unroll
      for (int k = 0; k < 10; ++k) sm += xv[k];
      const float mean = sum_hi(sum_lo(sm)) * (1.0f / D);
      float sv = 0.f;
#pragma unroll
      for (int k = 0; k < 10; ++k) { const float dlt = xv[k] - mean; sv = fmaf(dlt, dlt, sv); }
      const float var = sum_hi(sum_lo(sv)) * (1.0f / D);
      const float mb = rbf(mean), den = rbf(sqrtf(rbf(rbf(var) + 1e-5f)));
      const float rden = 1.0f / den;
      u32 xn[5];
#pragma unroll
      for (int k = 0; k < 5; ++k) { const u32 xr = k < 4 ? xo4[k] : xo1;
        xn[k] = pack2(rbf(asf(xr << 16) - mb) * rden, rbf(asf(xr & 0xffff0000u) - mb) * rden); PIN(xn[k]); }
      SB();
      float acc[8];
#pragma unroll
      for (int k = 0; k < 8; ++k) acc[k] = 0.f;
#pragma unroll
      for (int i = 0; i < 5; ++i) { mac2(acc, a[2 * i], a[2 * i + 1], xn[i]); SB(); }
      float o0, o1; red8(acc, o0, o1);
      const int dst = (w < 3) ? WS_QP + (h * 8 + s) * 1536 + w * 512 : WS_GU + ((w - 3) * 8 + rc) * 12288 + cb * 512;
      if (!(lane & 8)) stsc(rws, dst + pos * 16, o0, o1, tag);
    } else {
      tag = __builtin_amdgcn_readfirstlane(__builtin_amdgcn_raw_buffer_load_b32(rws, WS_E, 0, AUX_SC1)) + 1u;
    }
    if (w >= 3) lds_inc(lds + L_QCNT, lane);
    STAMP(2);
    u32x4 t[6];
    if (isd) {
      const rsrc_t rd = mk(wd, F * D * 2);
      const int voff = r * 8 * 1280 + (b & 1) * 640 + 128 * w + 16 * c;
#pragma unroll
      for (int i = 0; i < 6; ++i) { const int G = 3 * (b >> 1) + (i >> 1); t[i] = ldnt(rd, voff, ((G >> 2) * 64 + (G & 3) * 2 + (i & 1)) * 1280); }
      SB();
    }
    if (w < 3) {
      __builtin_amdgcn_s_sleep(QDELAY);
      const int src = WS_QP + (h * 8 + (lane >> 5)) * 1536 + w * 512 + (lane & 31) * 16;
      u32x4 g[4];
#pragma nounroll
      for (int it = 0; it < SPIN_CAP; ++it) {
        CFENCE();
#pragma unroll
        for (int i = 0; i < 4; ++i) g[i] = ldsc(rws, src, i * 3072);
        u32 e = 0;
#pragma unroll
        for (int i = 0; i < 4; ++i) e = bad(e, g[i], tag);
        if (all_zero(e)) break;
      }
      float q0 = (asf(g[0][0]) + asf(g[1][0])) + (asf(g[2][0]) + asf(g[3][0]));
      float q1 = (asf(g[0][2]) + asf(g[1][2])) + (asf(g[2][2]) + asf(g[3][2]));
      q0 = rs32(q0, q0); q1 = rs32(q1, q1);
      const int p = lane & 31;
      if (lane < 32) lds[L_Q + 32 * w + 4 * (p & 7) + (p >> 3)] = pack2(q0, q1);
      if (lane == 0) lds[L_TAG] = tag;
      lds_inc(lds + L_QCNT, lane);
    }
    STAMP(3);
    if (isd) {
      if (w == 3) {
        lds_wait_ge(vl + L_QCNT, 5u);
        const int src = WS_GU + r * 12288 + (3 * (b >> 1) * 8 + c) * 16;
        u32x4 g[6];
#pragma nounroll
        for (int it = 0; it < SPIN_CAP; ++it) {
          CFENCE();
#pragma unroll
          for (int i = 0; i < 6; ++i) g[i] = ldsc(rws, src + (i >> 1) * 128, (i & 1) * 8 * 12288);
          u32 e = 0;
#pragma unroll
          for (int i = 0; i < 6; ++i) e = bad(e, g[i], tag);
          if (all_zero(e)) break;
        }
        float ag[8] = {asf(g[0][0]), asf(g[0][2]), asf(g[2][0]), asf(g[2][2]), asf(g[4][0]), asf(g[4][2]), 0.f, 0.f};
        float au[8] = {asf(g[1][0]), asf(g[1][2]), asf(g[3][0]), asf(g[3][2]), asf(g[5][0]), asf(g[5][2]), 0.f, 0.f};
        float g0, g1, u0, u1; red8(ag, g0, g1); red8(au, u0, u1);
        if (lane < 48) lds[L_H + pos] = pack2(rbf(g0) * rbf(u0), rbf(g1) * rbf(u1));
        lds_inc(lds + L_HRDY, lane);
      }
      lds_wait_ge(vl + L_HRDY, 1u);
      float acc[8];
#pragma unroll
      for (int k = 0; k < 8; ++k) acc[k] = 0.f;
#pragma unroll
      for (int i = 0; i < 3; ++i) mac2(acc, t[2 * i], t[2 * i + 1], lds[L_H + 8 * i + r]);
      float o0, o1; red8(acc, o0, o1);
      if (!(lane & 8)) stsc(rws, WS_DP + (b >> 1) * 5120 + (b & 1) * 2560 + w * 512 + pos * 16, o0, o1, tag);
    }
    STAMP(4);
    lds_wait_ge(vl + L_VISS, 6u);
    {
      const rsrc_t ro = mk(wo, NQ * D * 2);
      const int voff = (h * 192 + r * 8) * 1280 + (s & 1) * 640 + 128 * w + 16 * c;
#pragma unroll
      for (int i = 0; i < 6; ++i) { const int G = 3 * (s >> 1) + (i >> 1); t[i] = ldnt(ro, voff, ((G >> 2) * 64 + (G & 3) * 2 + (i & 1)) * 1280); }
    }
    SB();
    STAMP(5);
    u32 pr[3][8];
#pragma unroll
    for (int i = 0; i < 3; ++i)
#pragma unroll
      for (int k = 0; k < 4; ++k) { pr[i][2 * k] = plo(t[2 * i][k], t[2 * i + 1][k]); pr[i][2 * k + 1] = phi(t[2 * i][k], t[2 * i + 1][k]); PIN(pr[i][2 * k]); PIN(pr[i][2 * k + 1]); }
    STAMP(6);
    lds_wait_ge(vl + L_ORDY, 1u);
    STAMP(7);
    {
      float acc[8];
#pragma unroll
      for (int k = 0; k < 8; ++k) acc[k] = 0.f;
#pragma unroll
      for (int i = 0; i < 3; ++i) { const u32 op = lds[L_O + 8 * i + r];
#pragma unroll
        for (int k = 0; k < 8; ++k) acc[k] = dot2(pr[i][k], op, acc[k]); }
      float o0, o1; red8(acc, o0, o1);
      if (!(lane & 8)) stsc(rws, WS_SP + (h * 4 + (s >> 1)) * 5120 + (s & 1) * 2560 + w * 512 + pos * 16, o0, o1, tag);
      if (lane == 0) vl[L_SDONE] = 1u;
      STAMP(8);
    }
  } else if (wave < 9) {
    const int kw = wave - 5;
    __builtin_amdgcn_s_setprio(2);
    const rsrc_t rk = mk(kc, 32u * SK * 384u);
    const int voff = (h * SK + (SK - WIN) + 256 * s + 64 * kw + r) * 384 + 16 * c;
    __builtin_amdgcn_s_barrier();
    u32x4 K[24];
#pragma unroll
    for (int i = 0; i < 24; ++i) { K[i] = ldnt(rk, voff + (i % 3) * 128, (i / 3) * 3072); if (i % 3 == 2) SB(); }
    STAMP(1);
    lds_wait_ge(vl + L_QCNT, 5u);
    STAMP(2);
    const u32 tag = __builtin_amdgcn_readfirstlane(vl[L_TAG]);
    u32x4 q[3];
#pragma unroll
    for (int t = 0; t < 3; ++t) q[t] = *(LAS u32x4*)(lds + L_Q + 32 * t + 4 * c);
    float sc[8];
#pragma unroll
    for (int g = 0; g < 8; ++g) {
      float d0 = 0.f;
#pragma unroll
      for (int t = 0; t < 3; ++t) { d0 = dot2(K[3 * g + t][0], q[t][0], d0); d0 = dot2(K[3 * g + t][1], q[t][1], d0); d0 = dot2(K[3 * g + t][2], q[t][2], d0); d0 = dot2(K[3 * g + t][3], q[t][3], d0); }
      sc[g] = d0; SB();
    }
#pragma unroll
    for (int g = 0; g < 8; ++g) sc[g] = rbf(rbf(sum_lo(sc[g])) * 0.07216878364870322f) * 1.4426950408889634f;
    const float mw = max_hi(fmaxf(fmaxf(fmaxf(sc[0], sc[1]), fmaxf(sc[2], sc[3])), fmaxf(fmaxf(sc[4], sc[5]), fmaxf(sc[6], sc[7]))));
    if (lane == 0) lds[L_KM + kw] = asu(mw);
    lds_inc(lds + L_KMC, lane);
    STAMP(3);
    lds_wait_ge(vl + L_KMC, 4u);
    const f32x4 m4 = *(LAS f32x4*)(lds + L_KM);
    const float M = fmaxf(fmaxf(m4[0], m4[1]), fmaxf(m4[2], m4[3]));
    float p[8];
#pragma unroll
    for (int g = 0; g < 8; ++g) p[g] = __builtin_amdgcn_exp2f(sc[g] - M);
    if (c == 0) *(LAS u32x4*)(lds + L_P + (kw >> 1) * 64 + r * 8 + (kw & 1) * 4) = u32x4{pack2(p[0], p[1]), pack2(p[2], p[3]), pack2(p[4], p[5]), pack2(p[6], p[7])};
    const float lw = sum_hi(((p[0] + p[1]) + (p[2] + p[3])) + ((p[4] + p[5]) + (p[6] + p[7])));
    if (lane == 0) lds[L_KL + kw] = asu(lw);
    if (lds_inc_rtn(lds + L_PCNT, lane) == 3u) {
      const f32x4 l4 = *(LAS f32x4*)(lds + L_KL);
      if (lane == 0) stsc(rws, WS_ML + (h * 8 + s) * 16, M, (l4[0] + l4[1]) + (l4[2] + l4[3]), tag);
    }
    STAMP(4);
    if (kw == 0) {
      __builtin_amdgcn_s_setprio(3);
      __builtin_amdgcn_s_sleep(MLDELAY);
      u32x4 ml;
#pragma nounroll
      for (int it = 0; it < SPIN_CAP; ++it) {
        CFENCE();
        ml = ldsc(rws, WS_ML + (h * 8 + r) * 16, 0);
        if (all_zero(bad(0u, ml, tag))) break;
      }
      const float m = asf(ml[0]), MM = max_hi(m);
      const float wj = __builtin_amdgcn_exp2f(m - MM);
      float wn = wj / sum_hi(wj * asf(ml[2]));
      PIN(wn);
      STAMP(5);
      lds_wait_ge(vl + L_OPUB, 3u);
      STAMP(6);
      const int src = WS_OP + (h * 8 + r) * 1536 + (24 * (s >> 1) + c) * 16;
      u32x4 g[3];
#pragma nounroll
      for (int it = 0; it < SPIN_CAP; ++it) {
        CFENCE();
#pragma unroll
        for (int i = 0; i < 3; ++i) g[i] = ldsc(rws, src + i * 128, 0);
        if (all_zero(bad(bad(bad(0u, g[0], tag), g[1], tag), g[2], tag))) break;
      }
      STAMP(7);
      float ao[8] = {wn * asf(g[0][0]), wn * asf(g[0][2]), wn * asf(g[1][0]), wn * asf(g[1][2]), wn * asf(g[2][0]), wn * asf(g[2][2]), 0.f, 0.f};
      float o0, o1; red8(ao, o0, o1);
      if (lane < 48) lds[L_O + pos] = pack2(o0, o1);
      lds_inc(lds + L_ORDY, lane);
      STAMP(8);
    } else if (kw == 1 && b >= 96) {
      const int tk = b - 96;
      const int ps = 2 * tk + (lane & 1), pw = ps % 160, p5 = pw & 31;
      const int col = (ps / 160) * 320 + (pw >> 5) * 64 + 8 * (p5 & 7) + 2 * (p5 >> 3) + (lane >> 5);
      const int so = (lane >> 1) * 5120 + ps * 16;
      lds_wait_ge(vl + L_VISS, 6u);
      float xv = asf((u32)x[col] << 16);
      u32x4 dg;
#pragma nounroll
      for (int it = 0; it < SPIN_CAP; ++it) {
        CFENCE();
        dg = ldsc(rws, WS_DP + so, 0);
        if (all_zero(bad(0u, dg, tag))) break;
      }
      float dv = rs32(asf(dg[0]), asf(dg[2]));
      dv = rs16(dv, dv); dv += dpp<ROR8>(dv); dv += dpp<ROR4>(dv); dv += dpp<XOR2>(dv);
      dv = rbf(dv);
      float gate = rbf(dv / (1.0f + __expf(-dv)));
      PIN(gate); PIN(xv);
      STAMP(5);
      lds_wait_ge(vl + L_SDONE, 1u);
      STAMP(6);
      u32x4 g[4];
#pragma nounroll
      for (int it = 0; it < SPIN_CAP; ++it) {
        CFENCE();
#pragma unroll
        for (int i = 0; i < 4; ++i) g[i] = ldsc(rws, WS_SP + so, i * 32 * 5120);
        u32 e = 0;
#pragma unroll
        for (int i = 0; i < 4; ++i) e = bad(e, g[i], tag);
        if (all_zero(e)) break;
      }
      STAMP(7);
      const float s0 = (asf(g[0][0]) + asf(g[1][0])) + (asf(g[2][0]) + asf(g[3][0]));
      const float s1 = (asf(g[0][2]) + asf(g[1][2])) + (asf(g[2][2]) + asf(g[3][2]));
      float sv = rs32(s0, s1);
      sv = rs16(sv, sv); sv += dpp<ROR8>(sv); sv += dpp<ROR4>(sv); sv += dpp<XOR2>(sv);
      const float res = xv + rbf(rbf(sv) * gate);
      if ((lane & 30) == 0) out[col] = __builtin_bit_cast(u16, (__bf16)res);
      if (tk == 0 && lane == 0) __builtin_amdgcn_raw_buffer_store_b32(tag, rws, WS_E, 0, AUX_SC1);
      STAMP(8);
    }
  } else {
    const int vw = wave - 9, half = vw >= 3, t = half ? vw - 3 : vw;
    __builtin_amdgcn_s_setprio(1);
    const rsrc_t rv = mk(vc, 32u * SK * 384u);
    const int voff = (h * SK + (SK - WIN) + 256 * s + 128 * half + r) * 384 + 128 * t + 16 * c;
    __builtin_amdgcn_s_barrier();
    u32x4 V[16];
#pragma unroll
    for (int i = 0; i < 16; ++i) { V[i] = ldnt(rv, voff, i * 3072); if (i & 1) SB(); }
    lds_inc(lds + L_VISS, lane);
    STAMP(1);
    lds_wait_ge(vl + L_PCNT, 4u);
    STAMP(2);
    const u32 tag = __builtin_amdgcn_readfirstlane(vl[L_TAG]);
    const u32x4 pa = *(LAS u32x4*)(lds + L_P + half * 64 + r * 8), pb = *(LAS u32x4*)(lds + L_P + half * 64 + r * 8 + 4);
    float acc[8];
#pragma unroll
    for (int k = 0; k < 8; ++k) acc[k] = 0.f;
#pragma unroll
    for (int n = 0; n < 7; ++n) { mac2(acc, V[2 * n], V[2 * n + 1], n < 4 ? pa[n] : pb[n - 4]); SB(); }
#pragma unroll
    for (int k = 0; k < 8; ++k) PIN(acc[k]);
    f32x2 rec = {0.f, 0.f};
    if (half) { lds_wait_ge(vl + L_VREC + t, 1u); rec = *(LAS f32x2*)(lds + L_REC + t * 64 + pos * 2); }
    PIN(rec);
    STAMP(3);
    mac2(acc, V[14], V[15], pb[3]);
    float o0, o1; red8(acc, o0, o1);
    if (half) {
      if (!(lane & 8)) stsc(rws, WS_OP + (h * 8 + s) * 1536 + t * 512 + pos * 16, o0 + rec[0], o1 + rec[1], tag);
      lds_inc(lds + L_OPUB, lane);
    } else {
      if (!(lane & 8)) *(LAS f32x2*)(lds + L_REC + t * 64 + pos * 2) = f32x2{o0, o1};
      lds_inc(lds + L_VREC + t, lane);
    }
    STAMP(4);
  }
  STAMP_FLUSH();
}

extern "C" void kernel_launch(void* const* d_in, const int* in_sizes, int n_in, void* d_out, int out_size, void* d_ws, size_t ws_size, hipStream_t stream) {
  fused_kernel<<<256, NWAVE * 64, 0, stream>>>((const u16*)d_in[0], (const u16*)d_in[1], (const u16*)d_in[2], (const u16*)d_in[3], (const u16*)d_in[4],
                                               (const u16*)d_in[6], (const u16*)d_in[7], (const u16*)d_in[8], (u16*)d_out, d_ws);
}
```

```cpp
#include <hip/hip_runtime.h>
typedef unsigned u32;

#define TILETAB2
struct TileTab32 { u32 t[32]; };
#define BUF(n) const void* p##n, u32 n##_bytes, u32 n##_fmt
__global__ void __launch_bounds__(1024, 4)
fused_kernel(BUF(x), BUF(ws), BUF(wd), BUF(wq), BUF(wo), BUF(wg), BUF(wu), BUF(k), BUF(out), BUF(v), TileTab32 tab) {
  __shared__ __attribute__((aligned(16))) u32 lds[1408];
  asm volatile(
    ".ifnc %0,s[0:1]\n"
    ".error \"kernarg register\"\n"
    ".endif\n"
    ".ifnc %1,s2\n"
    ".error \"workgroup id register\"\n"
    ".endif\n"
    ".ifnc %2,v0\n"
    ".error \"work-item id register\"\n"
    ".endif\n"
    ".Lentry:\n"
    "; ==== entry: buffer descriptors come ready-made in the kernel arguments; wave id picks the role ====\n"
    "; s44 wave, s45 head h, s46 split s, v0 lane, v1 r = lane>>3, v2 c = lane&7, s49 spin budget\n"
    "s_load_dwordx16 s[4:19], %0, 0\n"
    "s_getpc_b64 s[74:75]\n"
    ".Lpc0:\n"
    "s_add_u32 s74, s74, .Lpace-.Lpc0\n"
    "s_addc_u32 s75, s75, 0\n"
    "v_readfirstlane_b32 s44, v0\n"
    "v_and_b32 v0, 63, v0\n"
    "s_and_b32 s50, s2, 7\n"
    "s_bfe_u32 s51, s2, 0x20003\n"
    "s_lshr_b32 s46, s2, 5\n"
    "s_lshl2_add_u32 s45, s50, s51\n"
    "v_lshrrev_b32 v1, 3, v0\n"
    "v_and_b32 v2, 7, v0\n"
    "s_lshr_b32 s44, s44, 6\n"
    "v_lshlrev_b32 v5, 4, v2\n"
    "v_mov_b32 v4, 0\n"
    "v_mov_b32 v7, 1\n"
    "s_mov_b32 s49, 0x200000\n"
    "s_sub_u32 s63, s44, 1\n"
    "s_cmp_lt_u32 s63, 5\n"
    "s_cbranch_scc1 .Lw_issue\n"
    "s_cmp_eq_u32 s44, 0\n"
    "s_cbranch_scc1 .Lc_issue\n"
    "s_waitcnt lgkmcnt(0)\n"
    "s_memrealtime s[76:77]\n"
    "s_load_dwordx16 s[20:35], s[0:1], 64\n"
    "s_load_dwordx8 s[36:43], s[0:1], 128\n"
    "s_barrier\n"
    "s_branch .Lkv_issue\n"
    "; ---- pace: wait until the 100 MHz clock reaches s76 (1/16 ticks), then advance s76 by s77 (issue-rate limiter, speed only)\n"
    ".Lpace:\n"
    "s_memrealtime s[50:51]\n"
    "s_waitcnt lgkmcnt(0)\n"
    "s_lshl_b32 s50, s50, 4\n"
    "s_sub_u32 s50, s50, s76\n"
    "s_cmp_lt_i32 s50, 0\n"
    "s_cbranch_scc1 .Lpace\n"
    "s_add_u32 s76, s76, s77\n"
    "s_setpc_b64 vcc\n"
    "; ---- control wave: clear the LDS flags, request the epoch word and x\n"
    ".Lc_issue:\n"
    "v_mul_u32_u24 v11, 20, v0\n"
    "v_lshlrev_b32 v8, 3, v0\n"
    "v_mov_b32 v18, 0\n"
    "v_mov_b32 v19, 0\n"
    "ds_write_b64 v8, v[18:19]\n"
    "s_waitcnt lgkmcnt(0)\n"
    "s_memrealtime s[76:77]\n"
    "buffer_load_dword v10, off, s[8:11], 0 sc1\n"
    "buffer_load_dwordx4 v[12:15], v11, s[4:7], 0 offen\n"
    "buffer_load_dword v16, v11, s[4:7], 0 offen offset:16\n"
    "s_barrier\n"
    "s_branch .Lc_main\n"
    "; ---- W waves: request the first-phase weight tiles (80 rows x 64 columns each)\n"
    ".Lw_issue:\n"
    "s_setprio 3\n"
    "s_cmp_ge_u32 s46, 6\n"
    "s_cbranch_scc1 .Lw_d_issue\n"
    "s_cmp_eq_u32 s63, 4\n"
    "s_cbranch_scc1 .Lw_g_issue\n"
    "s_and_b32 s71, s45, 1\n"
    "s_mul_i32 s71, s71, 6\n"
    "s_add_u32 s71, s71, s46 ; idx12\n"
    "s_lshr_b32 s72, s45, 1\n"
    "s_mul_i32 s72, s72, 3\n"
    "s_lshr_b32 s73, s71, 2\n"
    "s_add_u32 s72, s72, s73 ; pair\n"
    "s_lshr_b32 s73, s63, 1\n"
    "s_lshl1_add_u32 s72, s72, s73 ; line\n"
    "s_lshl_b32 s72, s72, 7\n"
    "s_and_b32 s71, s71, 3\n"
    "s_and_b32 s73, s63, 1\n"
    "s_lshl1_add_u32 s71, s71, s73\n"
    "s_mul_i32 s66, s71, 80 ; q row base\n"
    "s_mul_i32 s57, s71, 983040\n"
    "s_add_u32 s57, s57, s72\n"
    "s_mov_b32 s58, 122880\n"
    "v_mad_u32_u24 v6, v1, s58, v5\n"
    "s_mov_b32 s68, 1\n"
    "s_waitcnt lgkmcnt(0)\n"
    "s_memrealtime s[76:77]\n"
    "buffer_load_dwordx4 v[64:67], v6, s[16:19], s57 offen nt\n"
    "s_addk_i32 s57, 12288\n"
    "buffer_load_dwordx4 v[68:71], v6, s[16:19], s57 offen nt\n"
    "s_addk_i32 s57, 12288\n"
    "s_barrier\n"
    "s_waitcnt lgkmcnt(0)\n"
    "s_lshl_b32 s3, s76, 4\n"
    "s_mul_i32 s76, s63, 118\n"
    "s_add_u32 s76, s76, s3\n"
    "s_sub_u32 s76, s76, 354\n"
    "s_movk_i32 s77, 590\n"
    "s_swappc_b64 vcc, s[74:75]\n"
    "buffer_load_dwordx4 v[72:75], v6, s[16:19], s57 offen nt\n"
    "s_addk_i32 s57, 12288\n"
    "buffer_load_dwordx4 v[76:79], v6, s[16:19], s57 offen nt\n"
    "s_addk_i32 s57, 12288\n"
    "s_swappc_b64 vcc, s[74:75]\n"
    "buffer_load_dwordx4 v[80:83], v6, s[16:19], s57 offen nt\n"
    "s_addk_i32 s57, 12288\n"
    "buffer_load_dwordx4 v[84:87], v6, s[16:19], s57 offen nt\n"
    "s_addk_i32 s57, 12288\n"
    "s_swappc_b64 vcc, s[74:75]\n"
    "buffer_load_dwordx4 v[88:91], v6, s[16:19], s57 offen nt\n"
    "s_addk_i32 s57, 12288\n"
    "buffer_load_dwordx4 v[92:95], v6, s[16:19], s57 offen nt\n"
    "s_addk_i32 s57, 12288\n"
    "s_swappc_b64 vcc, s[74:75]\n"
    "buffer_load_dwordx4 v[96:99], v6, s[16:19], s57 offen nt\n"
    "s_addk_i32 s57, 12288\n"
    "buffer_load_dwordx4 v[100:103], v6, s[16:19], s57 offen nt\n"
    "s_load_dwordx16 s[20:35], s[0:1], 64\n"
    "s_load_dwordx8 s[36:43], s[0:1], 128\n"
    "s_mov_b32 s69, 0\n"
    "s_branch .Lw_main\n"
    "; ---- splits 6, 7: a 48 x 320 tile of w_down, and gate/up tiles from the table\n"
    ".Lw_d_issue:\n"
    "s_load_dwordx16 s[20:35], s[0:1], 64\n"
    "s_load_dwordx8 s[36:43], s[0:1], 128\n"
    "s_lshr_b32 s69, s2, 3\n"
    "s_lshl2_add_u32 s69, s69, 160\n"
    "s_load_dword s69, s[0:1], s69\n"
    "s_movk_i32 s58, 10240\n"
    "v_mad_u32_u24 v6, v1, s58, v5\n"
    "s_and_b32 s70, s46, 1\n"
    "s_mul_i32 s71, s70, 5\n"
    "s_add_u32 s71, s71, s63\n"
    "s_lshl_b32 s57, s71, 7\n"
    "s_mul_i32 s72, s45, 3\n"
    "s_mul_i32 s70, s63, 10\n"
    "s_waitcnt lgkmcnt(0)\n"
    "s_memrealtime s[76:77]\n"
    "s_bitcmp1_b32 s2, 0\n"
    "s_cselect_b32 s58, 16, 0\n"
    "s_lshr_b32 s69, s69, s58\n"
    "s_bfe_u32 s58, s69, 0x70007\n"
    "s_and_b32 s69, s69, 0x7f\n"
    "s_lshl_b32 s58, s58, 10\n"
    "s_or_b32 s69, s69, s58\n"
    "s_lshl_b32 s69, s69, 3\n"
    "s_and_b32 s58, s2, 7\n"
    "s_mul_i32 s58, s58, 0x401\n"
    "s_or_b32 s69, s69, s58\n"
    "s_lshr_b32 s69, s69, s70\n"
    "s_and_b32 s69, s69, 0x3ff\n"
    "s_cmp_ge_u32 s63, 2\n"
    "s_cselect_b32 s69, 0, s69\n"
    "s_waitcnt lgkmcnt(0)\n"
    "s_lshl_b32 s3, s76, 4\n"
    "s_mul_i32 s76, s63, 118\n"
    "s_add_u32 s76, s76, s3\n"
    "s_sub_u32 s76, s76, 354\n"
    "s_movk_i32 s77, 590\n"
    "s_add_u32 s58, s72, 0\n"
    "s_lshr_b32 s59, s58, 2\n"
    "s_and_b32 s58, s58, 3\n"
    "s_lshl_b32 s59, s59, 5\n"
    "s_add_u32 s58, s58, s59\n"
    "s_mul_i32 s58, s58, 2560\n"
    "s_add_u32 s58, s58, s57\n"
    "buffer_load_dwordx4 v[32:35], v6, s[12:15], s58 offen nt\n"
    "buffer_load_dwordx4 v[36:39], v6, s[12:15], s58 offen offset:1280 nt\n"
    "s_barrier\n"
    "s_add_u32 s58, s72, 1\n"
    "s_lshr_b32 s59, s58, 2\n"
    "s_and_b32 s58, s58, 3\n"
    "s_lshl_b32 s59, s59, 5\n"
    "s_add_u32 s58, s58, s59\n"
    "s_mul_i32 s58, s58, 2560\n"
    "s_add_u32 s58, s58, s57\n"
    "s_swappc_b64 vcc, s[74:75]\n"
    "buffer_load_dwordx4 v[40:43], v6, s[12:15], s58 offen nt\n"
    "buffer_load_dwordx4 v[44:47], v6, s[12:15], s58 offen offset:1280 nt\n"
    "s_add_u32 s58, s72, 2\n"
    "s_lshr_b32 s59, s58, 2\n"
    "s_and_b32 s58, s58, 3\n"
    "s_lshl_b32 s59, s59, 5\n"
    "s_add_u32 s58, s58, s59\n"
    "s_mul_i32 s58, s58, 2560\n"
    "s_add_u32 s58, s58, s57\n"
    "s_swappc_b64 vcc, s[74:75]\n"
    "buffer_load_dwordx4 v[48:51], v6, s[12:15], s58 offen nt\n"
    "buffer_load_dwordx4 v[52:55], v6, s[12:15], s58 offen offset:1280 nt\n"
    "s_movk_i32 s77, 259\n"
    "s_branch .Lw_g2\n"
    ".Lw_g_issue:\n"
    "s_load_dwordx16 s[20:35], s[0:1], 64\n"
    "s_load_dwordx8 s[36:43], s[0:1], 128\n"
    "s_lshr_b32 s69, s2, 3\n"
    "s_lshl2_add_u32 s69, s69, 160\n"
    "s_load_dword s69, s[0:1], s69\n"
    "s_waitcnt lgkmcnt(0)\n"
    "s_memrealtime s[76:77]\n"
    "s_barrier\n"
    "s_bitcmp1_b32 s2, 0\n"
    "s_cselect_b32 s58, 16, 0\n"
    "s_lshr_b32 s69, s69, s58\n"
    "s_bfe_u32 s58, s69, 0x70007\n"
    "s_and_b32 s69, s69, 0x7f\n"
    "s_lshl_b32 s58, s58, 10\n"
    "s_or_b32 s69, s69, s58\n"
    "s_lshl_b32 s69, s69, 3\n"
    "s_and_b32 s58, s2, 7\n"
    "s_mul_i32 s58, s58, 0x401\n"
    "s_or_b32 s69, s69, s58\n"
    "s_waitcnt lgkmcnt(0)\n"
    "s_lshl_b32 s3, s76, 4\n"
    "s_sub_u32 s76, s3, 708\n"
    "s_bitcmp0_b32 s69, 19\n"
    "s_movk_i32 s77, 342\n"
    "s_cselect_b32 s77, 684, s77\n"
    ".Lw_g2:\n"
    "s_movk_i32 s58, 30720\n"
    "v_mad_u32_u24 v6, v1, s58, v5\n"
    "s_mov_b32 s68, 0\n"
    "s_mov_b64 s[54:55], s[26:27]\n"
    "s_bitcmp0_b32 s69, 9\n"
    "s_cbranch_scc1 .Lw_idle2\n"
    "s_and_b32 s58, s69, 7\n"
    "s_mul_i32 s57, s58, 245760\n"
    "s_bfe_u32 s59, s69, 0x50004\n"
    "s_lshl_b32 s59, s59, 7\n"
    "s_add_u32 s57, s57, s59\n"
    "s_bitcmp1_b32 s69, 3\n"
    "s_cselect_b64 s[52:53], s[28:29], s[24:25]\n"
    "s_swappc_b64 vcc, s[74:75]\n"
    "buffer_load_dwordx4 v[64:67], v6, s[52:55], s57 offen offset:0 nt\n"
    "buffer_load_dwordx4 v[68:71], v6, s[52:55], s57 offen offset:3072 nt\n"
    "s_addk_i32 s57, 6144\n"
    "s_swappc_b64 vcc, s[74:75]\n"
    "buffer_load_dwordx4 v[72:75], v6, s[52:55], s57 offen offset:0 nt\n"
    "buffer_load_dwordx4 v[76:79], v6, s[52:55], s57 offen offset:3072 nt\n"
    "s_addk_i32 s57, 6144\n"
    "s_swappc_b64 vcc, s[74:75]\n"
    "buffer_load_dwordx4 v[80:83], v6, s[52:55], s57 offen offset:0 nt\n"
    "buffer_load_dwordx4 v[84:87], v6, s[52:55], s57 offen offset:3072 nt\n"
    "s_addk_i32 s57, 6144\n"
    "s_swappc_b64 vcc, s[74:75]\n"
    "buffer_load_dwordx4 v[88:91], v6, s[52:55], s57 offen offset:0 nt\n"
    "buffer_load_dwordx4 v[92:95], v6, s[52:55], s57 offen offset:3072 nt\n"
    "s_addk_i32 s57, 6144\n"
    "s_swappc_b64 vcc, s[74:75]\n"
    "buffer_load_dwordx4 v[96:99], v6, s[52:55], s57 offen offset:0 nt\n"
    "buffer_load_dwordx4 v[100:103], v6, s[52:55], s57 offen offset:3072 nt\n"
    "s_mul_i32 s66, s58, 80\n"
    "s_bitcmp0_b32 s69, 19\n"
    "s_cbranch_scc1 .Lw_main\n"
    "s_mov_b32 s70, s69\n"
    "s_lshr_b32 s69, s69, 10\n"
    "s_and_b32 s58, s69, 7\n"
    "s_mul_i32 s57, s58, 245760\n"
    "s_bfe_u32 s59, s69, 0x50004\n"
    "s_lshl_b32 s59, s59, 7\n"
    "s_add_u32 s57, s57, s59\n"
    "s_bitcmp1_b32 s69, 3\n"
    "s_cselect_b64 s[52:53], s[28:29], s[24:25]\n"
    "s_swappc_b64 vcc, s[74:75]\n"
    "buffer_load_dwordx4 v[104:107], v6, s[52:55], s57 offen offset:0 nt\n"
    "buffer_load_dwordx4 v[108:111], v6, s[52:55], s57 offen offset:3072 nt\n"
    "s_addk_i32 s57, 6144\n"
    "s_swappc_b64 vcc, s[74:75]\n"
    "buffer_load_dwordx4 v[112:115], v6, s[52:55], s57 offen offset:0 nt\n"
    "buffer_load_dwordx4 v[116:119], v6, s[52:55], s57 offen offset:3072 nt\n"
    "s_addk_i32 s57, 6144\n"
    "s_swappc_b64 vcc, s[74:75]\n"
    "buffer_load_dwordx4 v[120:123], v6, s[52:55], s57 offen offset:0 nt\n"
    "buffer_load_dwordx4 v[124:127], v6, s[52:55], s57 offen offset:3072 nt\n"
    "s_addk_i32 s57, 6144\n"
    "s_swappc_b64 vcc, s[74:75]\n"
    "buffer_load_dwordx4 v[32:35], v6, s[52:55], s57 offen offset:0 nt\n"
    "buffer_load_dwordx4 v[36:39], v6, s[52:55], s57 offen offset:3072 nt\n"
    "s_addk_i32 s57, 6144\n"
    "s_swappc_b64 vcc, s[74:75]\n"
    "buffer_load_dwordx4 v[40:43], v6, s[52:55], s57 offen offset:0 nt\n"
    "buffer_load_dwordx4 v[44:47], v6, s[52:55], s57 offen offset:3072 nt\n"
    "s_mov_b32 s69, s70\n"
    "s_branch .Lw_main\n"
    "; ---- K waves (64 keys each) and V waves (128 keys x 64 dims each): paced streaming loads into registers\n"
    ".Lkv_issue:\n"
    "s_waitcnt lgkmcnt(0)\n"
    "s_lshl_b32 s3, s76, 4\n"
    "s_lshl_b32 s56, s45, 13\n"
    "s_mul_i32 s57, s46, 256\n"
    "s_add_u32 s56, s56, s57\n"
    "s_add_u32 s56, s56, 6144 ; first key of this workgroup\n"
    "s_movk_i32 s58, 384\n"
    "v_mad_u32_u24 v6, v1, s58, v5\n"
    "s_cmp_ge_u32 s44, 10\n"
    "s_cbranch_scc1 .Lv_issue\n"
    "s_sub_u32 s63, s44, 6 ; kw\n"
    "s_mul_i32 s57, s63, 64\n"
    "s_add_u32 s56, s56, s57\n"
    "s_mul_i32 s57, s56, 384\n"
    "s_setprio 2\n"
    "s_mul_i32 s76, s63, 177\n"
    "s_add_u32 s76, s76, s3\n"
    "s_add_u32 s76, s76, 2478\n"
    "s_movk_i32 s77, 708\n"
    "s_swappc_b64 vcc, s[74:75]\n"
    "buffer_load_dwordx4 v[32:35], v6, s[32:35], s57 offen offset:0 nt\n"
    "buffer_load_dwordx4 v[36:39], v6, s[32:35], s57 offen offset:128 nt\n"
    "buffer_load_dwordx4 v[40:43], v6, s[32:35], s57 offen offset:256 nt\n"
    "s_add_u32 s57, s57, 3072\n"
    "s_swappc_b64 vcc, s[74:75]\n"
    "buffer_load_dwordx4 v[44:47], v6, s[32:35], s57 offen offset:0 nt\n"
    "buffer_load_dwordx4 v[48:51], v6, s[32:35], s57 offen offset:128 nt\n"
    "buffer_load_dwordx4 v[52:55], v6, s[32:35], s57 offen offset:256 nt\n"
    "s_add_u32 s57, s57, 3072\n"
    "s_swappc_b64 vcc, s[74:75]\n"
    "buffer_load_dwordx4 v[56:59], v6, s[32:35], s57 offen offset:0 nt\n"
    "buffer_load_dwordx4 v[60:63], v6, s[32:35], s57 offen offset:128 nt\n"
    "buffer_load_dwordx4 v[64:67], v6, s[32:35], s57 offen offset:256 nt\n"
    "s_add_u32 s57, s57, 3072\n"
    "s_swappc_b64 vcc, s[74:75]\n"
    "buffer_load_dwordx4 v[68:71], v6, s[32:35], s57 offen offset:0 nt\n"
    "buffer_load_dwordx4 v[72:75], v6, s[32:35], s57 offen offset:128 nt\n"
    "buffer_load_dwordx4 v[76:79], v6, s[32:35], s57 offen offset:256 nt\n"
    "s_add_u32 s57, s57, 3072\n"
    "s_swappc_b64 vcc, s[74:75]\n"
    "buffer_load_dwordx4 v[80:83], v6, s[32:35], s57 offen offset:0 nt\n"
    "buffer_load_dwordx4 v[84:87], v6, s[32:35], s57 offen offset:128 nt\n"
    "buffer_load_dwordx4 v[88:91], v6, s[32:35], s57 offen offset:256 nt\n"
    "s_add_u32 s57, s57, 3072\n"
    "s_swappc_b64 vcc, s[74:75]\n"
    "buffer_load_dwordx4 v[92:95], v6, s[32:35], s57 offen offset:0 nt\n"
    "buffer_load_dwordx4 v[96:99], v6, s[32:35], s57 offen offset:128 nt\n"
    "buffer_load_dwordx4 v[100:103], v6, s[32:35], s57 offen offset:256 nt\n"
    "s_add_u32 s57, s57, 3072\n"
    "s_swappc_b64 vcc, s[74:75]\n"
    "buffer_load_dwordx4 v[104:107], v6, s[32:35], s57 offen offset:0 nt\n"
    "buffer_load_dwordx4 v[108:111], v6, s[32:35], s57 offen offset:128 nt\n"
    "buffer_load_dwordx4 v[112:115], v6, s[32:35], s57 offen offset:256 nt\n"
    "s_add_u32 s57, s57, 3072\n"
    "s_swappc_b64 vcc, s[74:75]\n"
    "buffer_load_dwordx4 v[116:119], v6, s[32:35], s57 offen offset:0 nt\n"
    "buffer_load_dwordx4 v[120:123], v6, s[32:35], s57 offen offset:128 nt\n"
    "buffer_load_dwordx4 v[124:127], v6, s[32:35], s57 offen offset:256 nt\n"
    "s_mov_b64 exec, 1\n"
    "ds_add_u32 v4, v7 offset:12\n"
    "s_mov_b64 exec, -1\n"
    "s_branch .Lk_main\n"
    ".Lv_issue:\n"
    "s_sub_u32 s63, s44, 10\n"
    "s_cmp_ge_u32 s63, 3\n"
    "s_cselect_b32 s66, 1, 0 ; half\n"
    "s_cselect_b32 s57, 3, 0\n"
    "s_sub_u32 s63, s63, s57 ; t\n"
    "s_mul_i32 s57, s66, 128\n"
    "s_add_u32 s56, s56, s57\n"
    "s_mul_i32 s57, s56, 384\n"
    "s_lshl_b32 s58, s63, 7\n"
    "s_add_u32 s57, s57, s58\n"
    "s_mov_b32 s60, 0x05040100\n"
    "s_mov_b32 s61, 0x07060302\n"
    "s_mov_b32 s62, 0xffff0000\n"
    "s_mov_b32 s64, 0x00ff00ff\n"
    "s_mov_b32 s65, 0x00ff00ff\n"
    "s_mov_b32 s78, 0xfffff000\n"
    "v_lshrrev_b32 v3, 4, v0\n"
    "v_lshl_or_b32 v3, v3, 3, v2\n"
    "v_lshlrev_b32 v3, 3, v3 ; pos*8\n"
    "v_mov_b32 v20, 0\n"
    "v_mov_b32 v21, 0\n"
    "v_mov_b32 v22, 0\n"
    "v_mov_b32 v23, 0\n"
    "v_mov_b32 v24, 0\n"
    "v_mov_b32 v25, 0\n"
    "v_mov_b32 v26, 0\n"
    "v_mov_b32 v27, 0\n"
    "s_lshl_b32 s56, s66, 8\n"
    "v_lshl_add_u32 v35, v1, 5, s56 ; P address: half*256 + r*32\n"
    "s_lshl_b32 s59, s63, 8\n"
    "v_add_u32 v11, s59, v3 ; REC address: t*256 + pos*8\n"
    "s_lshl_b32 s58, s63, 2\n"
    "v_mov_b32 v34, s58 ; VREC flag address\n"
    "s_lshl_b32 s58, s45, 3\n"
    "s_add_u32 s58, s58, s46\n"
    "s_mul_i32 s58, s58, 768\n"
    "s_add_u32 s70, s58, s59\n"
    "s_add_u32 s70, s70, 233472\n"
    "s_lshl_b32 s72, s63, 7\n"
    "s_sub_u32 s72, s57, s72\n"
    "s_mul_i32 s71, s66, 49088\n"
    "s_add_u32 s72, s72, s71 ; first (half 0) or last (half 1) line of the chunk\n"
    "s_add_u32 s71, s45, s66\n"
    "s_mul_i32 s71, s71, 245760\n"
    "s_lshl_b32 s69, s66, 6\n"
    "s_sub_u32 s71, s71, s69\n"
    "s_cmp_eq_u32 s63, 1\n"
    "s_cselect_b64 s[52:53], s[40:41], s[32:33]\n"
    "s_mov_b64 s[54:55], s[34:35]\n"
    "s_cmp_eq_u32 s63, 2\n"
    "s_cselect_b64 s[52:53], s[20:21], s[52:53]\n"
    "s_cselect_b64 s[54:55], s[22:23], s[54:55]\n"
    "s_cselect_b32 s72, s71, s72\n"
    "s_sleep 4\n"
    "s_buffer_load_dword s73, s[52:55], s72\n"
    "s_cmp_eq_u32 s66, 1\n"
    "s_cselect_b32 s58, 24, 12\n"
    "s_cselect_b32 s59, 3, 4\n"
    "v_mov_b32 v10, s58\n"
    ".Lv_gate:\n"
    "ds_read_b32 v8, v10\n"
    "s_waitcnt lgkmcnt(0)\n"
    "v_readfirstlane_b32 s56, v8\n"
    "s_cmp_ge_u32 s56, s59\n"
    "s_cbranch_scc1 .Lv_go\n"
    "s_sleep 1\n"
    "s_sub_u32 s49, s49, 1\n"
    "s_cbranch_scc0 .Lv_gate\n"
    "s_mov_b32 s49, 0\n"
    ".Lv_go:\n"
    "s_setprio 2\n"
    "s_waitcnt vmcnt(0)\n"
    "s_mul_i32 s58, s66, 48\n"
    "s_mul_i32 s59, s63, 0\n"
    "s_add_u32 s58, s58, s59\n"
    "ds_read_b32 v8, v4 offset:72\n"
    "s_waitcnt lgkmcnt(0)\n"
    "v_readfirstlane_b32 s56, v8\n"
    "s_cmp_lg_u32 s56, 0\n"
    "s_cselect_b32 s3, s56, s3\n"
    "s_mul_i32 s76, s58, 60\n"
    "s_add_u32 s76, s76, s3\n"
    "s_add_u32 s76, s76, 8142\n"
    "s_movk_i32 s77, 360\n"
    "s_swappc_b64 vcc, s[74:75]\n"
    "buffer_load_dwordx4 v[64:67], v6, s[40:43], s57 offen offset:0 nt\n"
    "buffer_load_dwordx4 v[68:71], v6, s[40:43], s57 offen offset:3072 nt\n"
    "s_swappc_b64 vcc, s[74:75]\n"
    "s_add_u32 s57, s57, 6144\n"
    "buffer_load_dwordx4 v[72:75], v6, s[40:43], s57 offen offset:0 nt\n"
    "buffer_load_dwordx4 v[76:79], v6, s[40:43], s57 offen offset:3072 nt\n"
    "s_swappc_b64 vcc, s[74:75]\n"
    "s_add_u32 s57, s57, 6144\n"
    "buffer_load_dwordx4 v[80:83], v6, s[40:43], s57 offen offset:0 nt\n"
    "buffer_load_dwordx4 v[84:87], v6, s[40:43], s57 offen offset:3072 nt\n"
    "s_swappc_b64 vcc, s[74:75]\n"
    "s_add_u32 s57, s57, 6144\n"
    "buffer_load_dwordx4 v[88:91], v6, s[40:43], s57 offen offset:0 nt\n"
    "buffer_load_dwordx4 v[92:95], v6, s[40:43], s57 offen offset:3072 nt\n"
    "s_swappc_b64 vcc, s[74:75]\n"
    "s_add_u32 s57, s57, 6144\n"
    "buffer_load_dwordx4 v[96:99], v6, s[40:43], s57 offen offset:0 nt\n"
    "buffer_load_dwordx4 v[100:103], v6, s[40:43], s57 offen offset:3072 nt\n"
    "s_swappc_b64 vcc, s[74:75]\n"
    "s_add_u32 s57, s57, 6144\n"
    "buffer_load_dwordx4 v[104:107], v6, s[40:43], s57 offen offset:0 nt\n"
    "buffer_load_dwordx4 v[108:111], v6, s[40:43], s57 offen offset:3072 nt\n"
    "s_swappc_b64 vcc, s[74:75]\n"
    "s_add_u32 s57, s57, 6144\n"
    "buffer_load_dwordx4 v[112:115], v6, s[40:43], s57 offen offset:0 nt\n"
    "buffer_load_dwordx4 v[116:119], v6, s[40:43], s57 offen offset:3072 nt\n"
    "s_swappc_b64 vcc, s[74:75]\n"
    "s_add_u32 s57, s57, 6144\n"
    "buffer_load_dwordx4 v[120:123], v6, s[40:43], s57 offen offset:0 nt\n"
    "buffer_load_dwordx4 v[124:127], v6, s[40:43], s57 offen offset:3072 nt\n"
    "s_mov_b64 exec, 1\n"
    "ds_add_u32 v4, v7 offset:24\n"
    "s_mov_b64 exec, -1\n"
    "; ==== V waves: o_partial = p . V; the late half adds the early half and publishes ====\n"
    "s_setprio 0\n"
    ".Lw_1:\n"
    "ds_read_b32 v8, v4 offset:20\n"
    "s_waitcnt lgkmcnt(0)\n"
    "v_readfirstlane_b32 s56, v8\n"
    "s_cmp_ge_u32 s56, 4\n"
    "s_cbranch_scc1 .Ld_2\n"
    "s_sleep 1\n"
    "s_sub_u32 s49, s49, 1\n"
    "s_cbranch_scc0 .Lw_1\n"
    "s_mov_b32 s49, 0\n"
    ".Ld_2:\n"
    "ds_read_b128 v[12:15], v35 offset:1024\n"
    "ds_read_b128 v[16:19], v35 offset:1040\n"
    "ds_read_b32 v8, v4 offset:64\n"
    "s_waitcnt lgkmcnt(0)\n"
    "v_readfirstlane_b32 s79, v8\n"
    "s_and_b32 s47, s79, 0xfff\n"
    "s_xor_b32 s48, s47, 0xa53\n"
    "v_mov_b32 v30, s47\n"
    "v_mov_b32 v31, s48\n"
    "s_waitcnt vmcnt(14)\n"
    "v_perm_b32 v8, v68, v64, s60\n"
    "v_dot2c_f32_bf16 v20, v8, v12\n"
    "v_perm_b32 v9, v68, v64, s61\n"
    "v_dot2c_f32_bf16 v21, v9, v12\n"
    "v_perm_b32 v8, v69, v65, s60\n"
    "v_dot2c_f32_bf16 v22, v8, v12\n"
    "v_perm_b32 v9, v69, v65, s61\n"
    "v_dot2c_f32_bf16 v23, v9, v12\n"
    "v_perm_b32 v8, v70, v66, s60\n"
    "v_dot2c_f32_bf16 v24, v8, v12\n"
    "v_perm_b32 v9, v70, v66, s61\n"
    "v_dot2c_f32_bf16 v25, v9, v12\n"
    "v_perm_b32 v8, v71, v67, s60\n"
    "v_dot2c_f32_bf16 v26, v8, v12\n"
    "v_perm_b32 v9, v71, v67, s61\n"
    "v_dot2c_f32_bf16 v27, v9, v12\n"
    "s_waitcnt vmcnt(12)\n"
    "v_perm_b32 v8, v76, v72, s60\n"
    "v_dot2c_f32_bf16 v20, v8, v13\n"
    "v_perm_b32 v9, v76, v72, s61\n"
    "v_dot2c_f32_bf16 v21, v9, v13\n"
    "v_perm_b32 v8, v77, v73, s60\n"
    "v_dot2c_f32_bf16 v22, v8, v13\n"
    "v_perm_b32 v9, v77, v73, s61\n"
    "v_dot2c_f32_bf16 v23, v9, v13\n"
    "v_perm_b32 v8, v78, v74, s60\n"
    "v_dot2c_f32_bf16 v24, v8, v13\n"
    "v_perm_b32 v9, v78, v74, s61\n"
    "v_dot2c_f32_bf16 v25, v9, v13\n"
    "v_perm_b32 v8, v79, v75, s60\n"
    "v_dot2c_f32_bf16 v26, v8, v13\n"
    "v_perm_b32 v9, v79, v75, s61\n"
    "v_dot2c_f32_bf16 v27, v9, v13\n"
    "s_waitcnt vmcnt(10)\n"
    "v_perm_b32 v8, v84, v80, s60\n"
    "v_dot2c_f32_bf16 v20, v8, v14\n"
    "v_perm_b32 v9, v84, v80, s61\n"
    "v_dot2c_f32_bf16 v21, v9, v14\n"
    "v_perm_b32 v8, v85, v81, s60\n"
    "v_dot2c_f32_bf16 v22, v8, v14\n"
    "v_perm_b32 v9, v85, v81, s61\n"
    "v_dot2c_f32_bf16 v23, v9, v14\n"
    "v_perm_b32 v8, v86, v82, s60\n"
    "v_dot2c_f32_bf16 v24, v8, v14\n"
    "v_perm_b32 v9, v86, v82, s61\n"
    "v_dot2c_f32_bf16 v25, v9, v14\n"
    "v_perm_b32 v8, v87, v83, s60\n"
    "v_dot2c_f32_bf16 v26, v8, v14\n"
    "v_perm_b32 v9, v87, v83, s61\n"
    "v_dot2c_f32_bf16 v27, v9, v14\n"
    "s_waitcnt vmcnt(8)\n"
    "v_perm_b32 v8, v92, v88, s60\n"
    "v_dot2c_f32_bf16 v20, v8, v15\n"
    "v_perm_b32 v9, v92, v88, s61\n"
    "v_dot2c_f32_bf16 v21, v9, v15\n"
    "v_perm_b32 v8, v93, v89, s60\n"
    "v_dot2c_f32_bf16 v22, v8, v15\n"
    "v_perm_b32 v9, v93, v89, s61\n"
    "v_dot2c_f32_bf16 v23, v9, v15\n"
    "v_perm_b32 v8, v94, v90, s60\n"
    "v_dot2c_f32_bf16 v24, v8, v15\n"
    "v_perm_b32 v9, v94, v90, s61\n"
    "v_dot2c_f32_bf16 v25, v9, v15\n"
    "v_perm_b32 v8, v95, v91, s60\n"
    "v_dot2c_f32_bf16 v26, v8, v15\n"
    "v_perm_b32 v9, v95, v91, s61\n"
    "v_dot2c_f32_bf16 v27, v9, v15\n"
    "s_waitcnt vmcnt(6)\n"
    "v_perm_b32 v8, v100, v96, s60\n"
    "v_dot2c_f32_bf16 v20, v8, v16\n"
    "v_perm_b32 v9, v100, v96, s61\n"
    "v_dot2c_f32_bf16 v21, v9, v16\n"
    "v_perm_b32 v8, v101, v97, s60\n"
    "v_dot2c_f32_bf16 v22, v8, v16\n"
    "v_perm_b32 v9, v101, v97, s61\n"
    "v_dot2c_f32_bf16 v23, v9, v16\n"
    "v_perm_b32 v8, v102, v98, s60\n"
    "v_dot2c_f32_bf16 v24, v8, v16\n"
    "v_perm_b32 v9, v102, v98, s61\n"
    "v_dot2c_f32_bf16 v25, v9, v16\n"
    "v_perm_b32 v8, v103, v99, s60\n"
    "v_dot2c_f32_bf16 v26, v8, v16\n"
    "v_perm_b32 v9, v103, v99, s61\n"
    "v_dot2c_f32_bf16 v27, v9, v16\n"
    "s_waitcnt vmcnt(4)\n"
    "v_perm_b32 v8, v108, v104, s60\n"
    "v_dot2c_f32_bf16 v20, v8, v17\n"
    "v_perm_b32 v9, v108, v104, s61\n"
    "v_dot2c_f32_bf16 v21, v9, v17\n"
    "v_perm_b32 v8, v109, v105, s60\n"
    "v_dot2c_f32_bf16 v22, v8, v17\n"
    "v_perm_b32 v9, v109, v105, s61\n"
    "v_dot2c_f32_bf16 v23, v9, v17\n"
    "v_perm_b32 v8, v110, v106, s60\n"
    "v_dot2c_f32_bf16 v24, v8, v17\n"
    "v_perm_b32 v9, v110, v106, s61\n"
    "v_dot2c_f32_bf16 v25, v9, v17\n"
    "v_perm_b32 v8, v111, v107, s60\n"
    "v_dot2c_f32_bf16 v26, v8, v17\n"
    "v_perm_b32 v9, v111, v107, s61\n"
    "v_dot2c_f32_bf16 v27, v9, v17\n"
    "s_waitcnt vmcnt(2)\n"
    "v_perm_b32 v8, v116, v112, s60\n"
    "v_dot2c_f32_bf16 v20, v8, v18\n"
    "v_perm_b32 v9, v116, v112, s61\n"
    "v_dot2c_f32_bf16 v21, v9, v18\n"
    "v_perm_b32 v8, v117, v113, s60\n"
    "v_dot2c_f32_bf16 v22, v8, v18\n"
    "v_perm_b32 v9, v117, v113, s61\n"
    "v_dot2c_f32_bf16 v23, v9, v18\n"
    "v_perm_b32 v8, v118, v114, s60\n"
    "v_dot2c_f32_bf16 v24, v8, v18\n"
    "v_perm_b32 v9, v118, v114, s61\n"
    "v_dot2c_f32_bf16 v25, v9, v18\n"
    "v_perm_b32 v8, v119, v115, s60\n"
    "v_dot2c_f32_bf16 v26, v8, v18\n"
    "v_perm_b32 v9, v119, v115, s61\n"
    "v_dot2c_f32_bf16 v27, v9, v18\n"
    "s_cmp_eq_u32 s66, 0\n"
    "s_cbranch_scc1 .Lv_early\n"
    ".Lv_rec:\n"
    "ds_read_b32 v8, v34 offset:28\n"
    "ds_read_b64 v[32:33], v11 offset:1536\n"
    "s_waitcnt lgkmcnt(0)\n"
    "v_readfirstlane_b32 s56, v8\n"
    "s_cmp_ge_u32 s56, 1\n"
    "s_cbranch_scc1 .Lv_rec_ok\n"
    "s_sub_u32 s49, s49, 1\n"
    "s_cbranch_scc0 .Lv_rec\n"
    "s_mov_b32 s49, 0\n"
    ".Lv_rec_ok:\n"
    "s_setprio 3\n"
    "s_waitcnt vmcnt(0)\n"
    "v_perm_b32 v8, v124, v120, s60\n"
    "v_dot2c_f32_bf16 v20, v8, v19\n"
    "v_perm_b32 v9, v124, v120, s61\n"
    "v_dot2c_f32_bf16 v21, v9, v19\n"
    "v_perm_b32 v8, v125, v121, s60\n"
    "v_dot2c_f32_bf16 v22, v8, v19\n"
    "v_perm_b32 v9, v125, v121, s61\n"
    "v_dot2c_f32_bf16 v23, v9, v19\n"
    "v_perm_b32 v8, v126, v122, s60\n"
    "v_dot2c_f32_bf16 v24, v8, v19\n"
    "v_perm_b32 v9, v126, v122, s61\n"
    "v_dot2c_f32_bf16 v25, v9, v19\n"
    "v_perm_b32 v8, v127, v123, s60\n"
    "v_dot2c_f32_bf16 v26, v8, v19\n"
    "v_perm_b32 v9, v127, v123, s61\n"
    "v_dot2c_f32_bf16 v27, v9, v19\n"
    "v_permlane32_swap_b32 v20, v24\n"
    "v_permlane32_swap_b32 v21, v25\n"
    "v_permlane32_swap_b32 v22, v26\n"
    "v_permlane32_swap_b32 v23, v27\n"
    "v_pk_add_f32 v[20:21], v[20:21], v[24:25]\n"
    "v_pk_add_f32 v[22:23], v[22:23], v[26:27]\n"
    "s_nop 1\n"
    "v_permlane16_swap_b32 v20, v22\n"
    "v_permlane16_swap_b32 v21, v23\n"
    "v_pk_add_f32 v[20:21], v[20:21], v[22:23]\n"
    "s_nop 1\n"
    "v_add_f32_dpp v28, v20, v20 row_ror:8 row_mask:0xf bank_mask:0xf bound_ctrl:1\n"
    "v_add_f32_dpp v29, v21, v21 row_ror:8 row_mask:0xf bank_mask:0xf bound_ctrl:1\n"
    "v_pk_add_f32 v[28:29], v[28:29], v[32:33]\n"
    "v_and_or_b32 v28, v28, s78, v30\n"
    "v_and_or_b32 v29, v29, s78, v31\n"
    "s_mov_b64 exec, s[64:65]\n"
    "buffer_store_dwordx2 v[28:29], v3, s[8:11], s70 offen sc1\n"
    "s_mov_b64 exec, -1\n"
    "s_mov_b64 exec, 1\n"
    "ds_add_u32 v4, v7 offset:40\n"
    "s_mov_b64 exec, -1\n"
    "s_cmp_eq_u32 s63, 1\n"
    "s_cbranch_scc1 .Lmaster\n"
    "s_add_u32 s63, s63, 7\n"
    "; ==== final reduction, 20 workgroups: 8 waves gather 16 partials of s each for 32 output columns ====\n"
    ".Lfinal:\n"
    "s_cmp_ge_u32 s63, 8\n"
    "s_cbranch_scc1 .Lf_end\n"
    "s_lshr_b32 s56, s2, 4\n"
    "s_and_b32 s57, s2, 7\n"
    "s_lshl3_add_u32 s56, s56, s57\n"
    "s_lshl_b32 s56, s56, 1\n"
    "s_bfe_u32 s57, s2, 0x10003\n"
    "s_add_u32 s2, s56, s57\n"
    "s_cmp_ge_u32 s2, 20\n"
    "s_cbranch_scc1 .Lf_end\n"
    "s_setprio 2\n"
    "s_movk_i32 s58, 2560\n"
    "v_lshrrev_b32 v9, 3, v0\n"
    "v_and_b32 v12, 7, v0\n"
    "v_lshlrev_b32 v13, 4, v12\n"
    "v_mad_u32_u24 v13, v9, s58, v13 ; producer*2560 + u*16\n"
    "s_mul_i32 s70, s63, 40960\n"
    "s_lshl_b32 s58, s2, 7\n"
    "s_add_u32 s70, s70, s58\n"
    "s_add_u32 s70, s70, 512000\n"
    "s_add_u32 s71, s70, 20480\n"
    "v_lshrrev_b32 v9, 4, v0\n"
    "v_lshl_add_u32 v9, v12, 2, v9 ; column j = 4u + 2*b5 + b4\n"
    "s_lshl_b32 s58, s63, 2\n"
    "v_lshl_add_u32 v12, v9, 5, s58 ; F address: j*32 + f*4\n"
    ".Lw_3:\n"
    "ds_read_b32 v8, v4 offset:44\n"
    "s_waitcnt lgkmcnt(0)\n"
    "v_readfirstlane_b32 s56, v8\n"
    "s_cmp_ge_u32 s56, 1\n"
    "s_cbranch_scc1 .Ld_4\n"
    "s_sleep 1\n"
    "s_sub_u32 s49, s49, 1\n"
    "s_cbranch_scc0 .Lw_3\n"
    "s_mov_b32 s49, 0\n"
    ".Ld_4:\n"
    "ds_read_b32 v8, v4 offset:72\n"
    "s_waitcnt lgkmcnt(0)\n"
    "v_readfirstlane_b32 s56, v8\n"
    "s_cmp_lg_u32 s56, 0\n"
    "s_cselect_b32 s3, s56, s3\n"
    "s_sleep 7\n"
    "s_add_u32 s76, s3, 17795\n"
    "s_swappc_b64 vcc, s[74:75]\n"
    ".Lp_5:\n"
    "buffer_load_dwordx4 v[16:19], v13, s[8:11], s70 offen offset:0 sc1\n"
    "buffer_load_dwordx4 v[20:23], v13, s[8:11], s71 offen offset:0 sc1\n"
    "s_waitcnt vmcnt(1)\n"
    "v_xor_b32 v8, s47, v16\n"
    "v_bitop3_b32 v8, v8, v17, s48 bitop3:0xf6\n"
    "v_bitop3_b32 v8, v8, v18, s47 bitop3:0xf6\n"
    "v_bitop3_b32 v8, v8, v19, s48 bitop3:0xf6\n"
    "s_waitcnt vmcnt(0)\n"
    "v_bitop3_b32 v8, v8, v20, s47 bitop3:0xf6\n"
    "v_bitop3_b32 v8, v8, v21, s48 bitop3:0xf6\n"
    "v_bitop3_b32 v8, v8, v22, s47 bitop3:0xf6\n"
    "v_bitop3_b32 v8, v8, v23, s48 bitop3:0xf6\n"
    "v_and_b32 v8, 0xfff, v8\n"
    "v_cmp_ne_u32 vcc, 0, v8\n"
    "s_cmp_eq_u64 vcc, 0\n"
    "s_cbranch_scc1 .Lpd_6\n"
    "s_sub_u32 s49, s49, 1\n"
    "s_cbranch_scc0 .Lp_5\n"
    "s_mov_b32 s49, 0\n"
    ".Lpd_6:\n"
    "v_and_b32 v16, s78, v16\n"
    "v_and_b32 v17, s78, v17\n"
    "v_and_b32 v18, s78, v18\n"
    "v_and_b32 v19, s78, v19\n"
    "v_and_b32 v20, s78, v20\n"
    "v_and_b32 v21, s78, v21\n"
    "v_and_b32 v22, s78, v22\n"
    "v_and_b32 v23, s78, v23\n"
    "v_pk_add_f32 v[16:17], v[16:17], v[20:21]\n"
    "v_pk_add_f32 v[18:19], v[18:19], v[22:23]\n"
    "s_nop 1\n"
    "v_permlane32_swap_b32 v16, v18\n"
    "v_permlane32_swap_b32 v17, v19\n"
    "v_pk_add_f32 v[16:17], v[16:17], v[18:19]\n"
    "s_nop 1\n"
    "v_permlane16_swap_b32 v16, v17\n"
    "v_add_f32 v16, v16, v17\n"
    "s_nop 1\n"
    "v_add_f32_dpp v16, v16, v16 row_ror:8 row_mask:0xf bank_mask:0xf bound_ctrl:1\n"
    "ds_write_b32 v12, v16 offset:4608\n"
    "s_mov_b64 exec, 1\n"
    "ds_add_u32 v4, v7 offset:48\n"
    "s_mov_b64 exec, -1\n"
    ".Lf_end:\n"
    "s_endpgm\n"
    ".p2align 12\n"
    "; ==== control wave: layer norm (bf16 rounding points of the reference), q exchange, softmax merge weights, o gather ====\n"
    ".Lc_main:\n"
    "s_setprio 3\n"
    "s_waitcnt lgkmcnt(0)\n"
    "s_lshl_b32 s3, s76, 4\n"
    "s_mov_b32 s60, 0x05040100\n"
    "s_mov_b32 s61, 0x07060302\n"
    "s_mov_b32 s62, 0xffff0000\n"
    "s_mov_b32 s64, 0x00ff00ff\n"
    "s_mov_b32 s65, 0x00ff00ff\n"
    "s_mov_b32 s78, 0xfffff000\n"
    "v_lshrrev_b32 v3, 4, v0\n"
    "v_lshl_or_b32 v3, v3, 3, v2\n"
    "v_lshlrev_b32 v3, 3, v3 ; pos*8\n"
    "s_waitcnt vmcnt(0)\n"
    "v_readfirstlane_b32 s79, v10\n"
    "s_add_u32 s79, s79, 1\n"
    "s_and_b32 s47, s79, 0xfff\n"
    "s_xor_b32 s48, s47, 0xa53\n"
    "v_lshlrev_b32 v20, 16, v12\n"
    "v_and_b32 v21, s62, v12\n"
    "v_lshlrev_b32 v22, 16, v13\n"
    "v_and_b32 v23, s62, v13\n"
    "v_lshlrev_b32 v24, 16, v14\n"
    "v_and_b32 v25, s62, v14\n"
    "v_lshlrev_b32 v26, 16, v15\n"
    "v_and_b32 v27, s62, v15\n"
    "v_lshlrev_b32 v28, 16, v16\n"
    "v_and_b32 v29, s62, v16\n"
    "v_pk_add_f32 v[30:31], v[20:21], v[22:23]\n"
    "v_pk_add_f32 v[32:33], v[24:25], v[26:27]\n"
    "v_pk_add_f32 v[30:31], v[30:31], v[28:29]\n"
    "v_pk_add_f32 v[30:31], v[30:31], v[32:33]\n"
    "v_add_f32 v30, v30, v31\n"
    "s_nop 1\n"
    "v_add_f32_dpp v30, v30, v30 quad_perm:[1,0,3,2] row_mask:0xf bank_mask:0xf bound_ctrl:1\n"
    "s_nop 1\n"
    "v_add_f32_dpp v30, v30, v30 quad_perm:[2,3,0,1] row_mask:0xf bank_mask:0xf bound_ctrl:1\n"
    "s_nop 1\n"
    "v_add_f32_dpp v30, v30, v30 row_half_mirror row_mask:0xf bank_mask:0xf bound_ctrl:1\n"
    "s_nop 1\n"
    "v_add_f32_dpp v30, v30, v30 row_ror:8 row_mask:0xf bank_mask:0xf bound_ctrl:1\n"
    "v_mov_b32 v31, v30\n"
    "s_nop 1\n"
    "v_permlane16_swap_b32 v30, v31\n"
    "v_add_f32 v30, v30, v31\n"
    "v_mov_b32 v31, v30\n"
    "s_nop 1\n"
    "v_permlane32_swap_b32 v30, v31\n"
    "v_add_f32 v30, v30, v31\n"
    "v_mul_f32 v30, 0x3acccccd, v30\n"
    "v_sub_f32 v32, v20, v30\n"
    "v_mul_f32 v33, v32, v32\n"
    "v_sub_f32 v32, v21, v30\n"
    "v_fmac_f32 v33, v32, v32\n"
    "v_sub_f32 v32, v22, v30\n"
    "v_fmac_f32 v33, v32, v32\n"
    "v_sub_f32 v32, v23, v30\n"
    "v_fmac_f32 v33, v32, v32\n"
    "v_sub_f32 v32, v24, v30\n"
    "v_fmac_f32 v33, v32, v32\n"
    "v_sub_f32 v32, v25, v30\n"
    "v_fmac_f32 v33, v32, v32\n"
    "v_sub_f32 v32, v26, v30\n"
    "v_fmac_f32 v33, v32, v32\n"
    "v_sub_f32 v32, v27, v30\n"
    "v_fmac_f32 v33, v32, v32\n"
    "v_sub_f32 v32, v28, v30\n"
    "v_fmac_f32 v33, v32, v32\n"
    "v_sub_f32 v32, v29, v30\n"
    "v_fmac_f32 v33, v32, v32\n"
    "s_nop 1\n"
    "v_add_f32_dpp v33, v33, v33 quad_perm:[1,0,3,2] row_mask:0xf bank_mask:0xf bound_ctrl:1\n"
    "s_nop 1\n"
    "v_add_f32_dpp v33, v33, v33 quad_perm:[2,3,0,1] row_mask:0xf bank_mask:0xf bound_ctrl:1\n"
    "s_nop 1\n"
    "v_add_f32_dpp v33, v33, v33 row_half_mirror row_mask:0xf bank_mask:0xf bound_ctrl:1\n"
    "s_nop 1\n"
    "v_add_f32_dpp v33, v33, v33 row_ror:8 row_mask:0xf bank_mask:0xf bound_ctrl:1\n"
    "v_mov_b32 v31, v33\n"
    "s_nop 1\n"
    "v_permlane16_swap_b32 v33, v31\n"
    "v_add_f32 v33, v33, v31\n"
    "v_mov_b32 v31, v33\n"
    "s_nop 1\n"
    "v_permlane32_swap_b32 v33, v31\n"
    "v_add_f32 v33, v33, v31\n"
    "v_mul_f32 v33, 0x3acccccd, v33\n"
    "v_cvt_pk_bf16_f32 v30, v30, v30\n"
    "v_and_b32 v30, s62, v30\n"
    "v_cvt_pk_bf16_f32 v33, v33, v33\n"
    "v_and_b32 v33, s62, v33\n"
    "v_add_f32 v33, 0x3727c5ac, v33\n"
    "v_cvt_pk_bf16_f32 v33, v33, v33\n"
    "v_and_b32 v33, s62, v33\n"
    "v_sqrt_f32 v33, v33\n"
    "s_nop 0\n"
    "v_cvt_pk_bf16_f32 v33, v33, v33\n"
    "v_and_b32 v33, s62, v33\n"
    "v_rcp_f32 v33, v33\n"
    "v_sub_f32 v20, v20, v30\n"
    "v_sub_f32 v21, v21, v30\n"
    "v_cvt_pk_bf16_f32 v31, v20, v21\n"
    "v_lshlrev_b32 v32, 16, v31\n"
    "v_and_b32 v31, s62, v31\n"
    "v_mul_f32 v32, v32, v33\n"
    "v_mul_f32 v31, v31, v33\n"
    "v_cvt_pk_bf16_f32 v12, v32, v31\n"
    "v_sub_f32 v22, v22, v30\n"
    "v_sub_f32 v23, v23, v30\n"
    "v_cvt_pk_bf16_f32 v31, v22, v23\n"
    "v_lshlrev_b32 v32, 16, v31\n"
    "v_and_b32 v31, s62, v31\n"
    "v_mul_f32 v32, v32, v33\n"
    "v_mul_f32 v31, v31, v33\n"
    "v_cvt_pk_bf16_f32 v13, v32, v31\n"
    "v_sub_f32 v24, v24, v30\n"
    "v_sub_f32 v25, v25, v30\n"
    "v_cvt_pk_bf16_f32 v31, v24, v25\n"
    "v_lshlrev_b32 v32, 16, v31\n"
    "v_and_b32 v31, s62, v31\n"
    "v_mul_f32 v32, v32, v33\n"
    "v_mul_f32 v31, v31, v33\n"
    "v_cvt_pk_bf16_f32 v14, v32, v31\n"
    "v_sub_f32 v26, v26, v30\n"
    "v_sub_f32 v27, v27, v30\n"
    "v_cvt_pk_bf16_f32 v31, v26, v27\n"
    "v_lshlrev_b32 v32, 16, v31\n"
    "v_and_b32 v31, s62, v31\n"
    "v_mul_f32 v32, v32, v33\n"
    "v_mul_f32 v31, v31, v33\n"
    "v_cvt_pk_bf16_f32 v15, v32, v31\n"
    "v_sub_f32 v28, v28, v30\n"
    "v_sub_f32 v29, v29, v30\n"
    "v_cvt_pk_bf16_f32 v31, v28, v29\n"
    "v_lshlrev_b32 v32, 16, v31\n"
    "v_and_b32 v31, s62, v31\n"
    "v_mul_f32 v32, v32, v33\n"
    "v_mul_f32 v31, v31, v33\n"
    "v_cvt_pk_bf16_f32 v16, v32, v31\n"
    "v_add_u32 v11, 2304, v11\n"
    "v_mov_b32 v8, s79\n"
    "ds_write2_b32 v11, v12, v13 offset0:0 offset1:1\n"
    "ds_write2_b32 v11, v14, v15 offset0:2 offset1:3\n"
    "ds_write_b32 v11, v16 offset:16\n"
    "ds_write_b32 v4, v8 offset:64\n"
    "s_mov_b64 exec, 1\n"
    "ds_add_u32 v4, v7 offset:0\n"
    "s_mov_b64 exec, -1\n"
    "v_mov_b32 v30, s47\n"
    "v_mov_b32 v31, s48\n"
    "; ---- workgroups 0..7 publish their start time (common pacing origin, speed only)\n"
    "s_cmp_ge_u32 s2, 8\n"
    "s_cbranch_scc1 .Lc_nt\n"
    "s_lshl_b32 s56, s3, 8\n"
    "v_mov_b32 v28, s56\n"
    "v_mov_b32 v29, s56\n"
    "s_lshl_b32 s57, s2, 7\n"
    "s_add_u32 s57, s57, 128\n"
    "v_lshlrev_b32 v9, 3, v0\n"
    "v_and_or_b32 v28, v28, s78, v30\n"
    "v_and_or_b32 v29, v29, s78, v31\n"
    "s_mov_b64 exec, 0xff\n"
    "buffer_store_dwordx2 v[28:29], v9, s[8:11], s57 offen sc1\n"
    "s_mov_b64 exec, -1\n"
    ".Lc_nt:\n"
    "v_and_b32 v30, 0xff, v30\n"
    "v_and_b32 v31, 0xff, v31\n"
    "s_mov_b32 s78, 0xffffff00\n"
    "; ---- sum the four W partials of q and publish them\n"
    "v_and_b32 v12, 31, v0 ; p\n"
    "v_lshrrev_b32 v13, 5, v0 ; hi\n"
    "s_cmp_ge_u32 s46, 6\n"
    "s_cbranch_scc1 .Lc_noq\n"
    "v_lshlrev_b32 v14, 3, v12\n"
    "v_lshl_add_u32 v14, v13, 9, v14 ; LDS: hi*512 + p*8\n"
    "v_lshlrev_b32 v15, 3, v12\n"
    "v_lshl_add_u32 v15, v13, 10, v15 ; ws: hi*1024 + p*8\n"
    "s_and_b32 s71, s45, 1\n"
    "s_mul_i32 s71, s71, 6\n"
    "s_add_u32 s71, s71, s46 ; idx12\n"
    "s_lshr_b32 s72, s45, 1\n"
    "s_mul_i32 s72, s72, 3\n"
    "s_lshr_b32 s73, s71, 2\n"
    "s_add_u32 s72, s72, s73 ; pair\n"
    "s_lshl_b32 s72, s72, 11\n"
    "s_and_b32 s71, s71, 3\n"
    "s_lshl_b32 s71, s71, 8\n"
    "s_add_u32 s72, s72, s71\n"
    "s_add_u32 s72, s72, 36864\n"
    "v_add_u32 v14, 3584, v14\n"
    ".Lw_7:\n"
    "ds_read_b32 v8, v4 offset:4\n"
    "s_waitcnt lgkmcnt(0)\n"
    "v_readfirstlane_b32 s56, v8\n"
    "s_cmp_ge_u32 s56, 4\n"
    "s_cbranch_scc1 .Ld_8\n"
    "s_sleep 1\n"
    "s_sub_u32 s49, s49, 1\n"
    "s_cbranch_scc0 .Lw_7\n"
    "s_mov_b32 s49, 0\n"
    ".Ld_8:\n"
    "ds_read2_b64 v[20:23], v14 offset0:0 offset1:32\n"
    "s_waitcnt lgkmcnt(0)\n"
    "v_pk_add_f32 v[28:29], v[20:21], v[22:23]\n"
    "v_add_u32 v28, 0x80, v28\n"
    "v_add_u32 v29, 0x80, v29\n"
    "v_and_or_b32 v28, v28, s78, v30\n"
    "v_and_or_b32 v29, v29, s78, v31\n"
    "buffer_store_dwordx2 v[28:29], v15, s[8:11], s72 offen sc1\n"
    ".Lc_noq:\n"
    "; ---- gather the q partials of this head, round q to bf16, hand it to the K waves\n"
    "v_lshlrev_b32 v15, 4, v0\n"
    "s_mul_i32 s72, s45, 3072\n"
    "s_add_u32 s72, s72, 36864\n"
    "v_and_b32 v16, 15, v0\n"
    "v_lshl_add_u32 v16, v16, 1, v13 ; p = 2u + b5\n"
    "v_and_b32 v17, 7, v16\n"
    "v_lshrrev_b32 v16, 3, v16\n"
    "v_lshl_add_u32 v16, v17, 2, v16\n"
    "v_lshlrev_b32 v16, 2, v16 ; (4*(p&7) + (p>>3))*4\n"
    "v_and_b32 v17, 16, v0\n"
    "v_lshl_add_u32 v17, v17, 3, v16 ; + b4*128\n"
    "s_add_u32 s76, s3, 6526\n"
    "s_swappc_b64 vcc, s[74:75]\n"
    "v_lshlrev_b32 v9, 3, v2\n"
    "v_lshl_add_u32 v9, v1, 7, v9\n"
    "buffer_load_dwordx2 v[46:47], v9, s[8:11], 0 offen offset:128 sc1\n"
    ".Lp_9:\n"
    "buffer_load_dwordx4 v[32:35], v15, s[8:11], s72 offen offset:0 sc1\n"
    "buffer_load_dwordx4 v[36:39], v15, s[8:11], s72 offen offset:1024 sc1\n"
    "buffer_load_dwordx4 v[40:43], v15, s[8:11], s72 offen offset:2048 sc1\n"
    "s_waitcnt vmcnt(2)\n"
    "v_xor_b32 v8, s47, v32\n"
    "v_bitop3_b32 v8, v8, v33, s48 bitop3:0xf6\n"
    "v_bitop3_b32 v8, v8, v34, s47 bitop3:0xf6\n"
    "v_bitop3_b32 v8, v8, v35, s48 bitop3:0xf6\n"
    "s_waitcnt vmcnt(1)\n"
    "v_bitop3_b32 v8, v8, v36, s47 bitop3:0xf6\n"
    "v_bitop3_b32 v8, v8, v37, s48 bitop3:0xf6\n"
    "v_bitop3_b32 v8, v8, v38, s47 bitop3:0xf6\n"
    "v_bitop3_b32 v8, v8, v39, s48 bitop3:0xf6\n"
    "s_waitcnt vmcnt(0)\n"
    "v_bitop3_b32 v8, v8, v40, s47 bitop3:0xf6\n"
    "v_bitop3_b32 v8, v8, v41, s48 bitop3:0xf6\n"
    "v_bitop3_b32 v8, v8, v42, s47 bitop3:0xf6\n"
    "v_bitop3_b32 v8, v8, v43, s48 bitop3:0xf6\n"
    "v_and_b32 v8, 0xff, v8\n"
    "v_cmp_ne_u32 vcc, 0, v8\n"
    "s_cmp_eq_u64 vcc, 0\n"
    "s_cbranch_scc1 .Lpd_10\n"
    "s_sub_u32 s49, s49, 1\n"
    "s_cbranch_scc0 .Lp_9\n"
    "s_mov_b32 s49, 0\n"
    ".Lpd_10:\n"
    "v_and_b32 v32, s78, v32\n"
    "v_and_b32 v33, s78, v33\n"
    "v_and_b32 v34, s78, v34\n"
    "v_and_b32 v35, s78, v35\n"
    "v_and_b32 v36, s78, v36\n"
    "v_and_b32 v37, s78, v37\n"
    "v_and_b32 v38, s78, v38\n"
    "v_and_b32 v39, s78, v39\n"
    "v_and_b32 v40, s78, v40\n"
    "v_and_b32 v41, s78, v41\n"
    "v_and_b32 v42, s78, v42\n"
    "v_and_b32 v43, s78, v43\n"
    "v_mov_b32 v30, s47\n"
    "v_mov_b32 v31, s48\n"
    "s_mov_b32 s78, 0xfffff000\n"
    "v_permlane32_swap_b32 v32, v34\n"
    "v_permlane32_swap_b32 v33, v35\n"
    "v_permlane32_swap_b32 v36, v38\n"
    "v_permlane32_swap_b32 v37, v39\n"
    "v_permlane32_swap_b32 v40, v42\n"
    "v_permlane32_swap_b32 v41, v43\n"
    "v_pk_add_f32 v[32:33], v[32:33], v[34:35]\n"
    "v_pk_add_f32 v[36:37], v[36:37], v[38:39]\n"
    "v_pk_add_f32 v[40:41], v[40:41], v[42:43]\n"
    "v_mov_b32 v42, v40\n"
    "v_mov_b32 v43, v41\n"
    "v_permlane16_swap_b32 v32, v36\n"
    "v_permlane16_swap_b32 v33, v37\n"
    "v_permlane16_swap_b32 v40, v42\n"
    "v_permlane16_swap_b32 v41, v43\n"
    "v_pk_add_f32 v[32:33], v[32:33], v[36:37]\n"
    "v_pk_add_f32 v[40:41], v[40:41], v[42:43]\n"
    "v_cvt_pk_bf16_f32 v32, v32, v33\n"
    "v_cvt_pk_bf16_f32 v40, v40, v41\n"
    "ds_write_b32 v17, v32 offset:512\n"
    "ds_write_b32 v16, v40 offset:768\n"
    "s_mov_b64 exec, 1\n"
    "ds_add_u32 v4, v7 offset:8\n"
    "s_mov_b64 exec, -1\n"
    "v_xor_b32 v8, s47, v46\n"
    "v_bitop3_b32 v8, v8, v47, s48 bitop3:0xf6\n"
    "v_and_b32 v8, 0xfff, v8\n"
    "v_cmp_ne_u32 vcc, 0, v8\n"
    "s_cmp_lg_u64 vcc, 0\n"
    "s_cbranch_scc1 .Lc_ns\n"
    "s_lshl_b32 s56, s3, 8\n"
    "v_and_b32 v46, 0xfffff000, v46\n"
    "v_subrev_u32 v46, s56, v46\n"
    "v_ashrrev_i32 v46, 12, v46\n"
    "v_cvt_f32_i32 v46, v46\n"
    "s_nop 1\n"
    "v_add_f32_dpp v46, v46, v46 row_ror:8 row_mask:0xf bank_mask:0xf bound_ctrl:1\n"
    "v_mov_b32 v47, v46\n"
    "s_nop 1\n"
    "v_permlane16_swap_b32 v46, v47\n"
    "v_add_f32 v46, v46, v47\n"
    "v_mov_b32 v47, v46\n"
    "s_nop 1\n"
    "v_permlane32_swap_b32 v46, v47\n"
    "v_add_f32 v46, v46, v47\n"
    "v_mul_f32 v46, 2.0, v46\n"
    "v_cvt_i32_f32 v46, v46\n"
    "s_nop 0\n"
    "v_readfirstlane_b32 s56, v46\n"
    "s_add_i32 s56, s56, 200\n"
    "s_min_i32 s56, s56, 800\n"
    "s_max_i32 s56, s56, -800\n"
    "s_add_u32 s3, s3, s56\n"
    ".Lc_ns:\n"
    "v_mov_b32 v8, s3\n"
    "ds_write_b32 v4, v8 offset:72\n"
    "; ---- publish this split max and sum; gather all eight; weight = exp(m - M) / L\n"
    "s_lshl_b32 s72, s45, 10\n"
    "s_add_u32 s72, s72, 4096\n"
    "s_lshl_b32 s73, s46, 7\n"
    "s_add_u32 s73, s73, s72\n"
    "v_lshlrev_b32 v16, 3, v2\n"
    "v_lshl_add_u32 v15, v1, 7, v16 ; r*128 + c*8 (eight copies per record)\n"
    "s_movk_i32 s58, 768\n"
    "v_mad_u32_u24 v16, v1, s58, v16 ; r*768 + c*8\n"
    "s_mul_i32 s70, s45, 6144\n"
    "s_lshr_b32 s58, s46, 1\n"
    "s_mul_i32 s58, s58, 192\n"
    "s_add_u32 s70, s70, s58\n"
    "s_add_u32 s70, s70, 233472\n"
    "v_lshrrev_b32 v17, 4, v0\n"
    "v_lshl_add_u32 v17, v17, 2, v5 ; O address: c*16 + i*4\n"
    "v_mov_b32 v26, 0\n"
    "v_mov_b32 v27, 0\n"
    ".Lw_11:\n"
    "ds_read_b32 v8, v4 offset:20\n"
    "s_waitcnt lgkmcnt(0)\n"
    "v_readfirstlane_b32 s56, v8\n"
    "s_cmp_ge_u32 s56, 4\n"
    "s_cbranch_scc1 .Ld_12\n"
    "s_sleep 1\n"
    "s_sub_u32 s49, s49, 1\n"
    "s_cbranch_scc0 .Lw_11\n"
    "s_mov_b32 s49, 0\n"
    ".Ld_12:\n"
    "ds_read_b128 v[32:35], v4 offset:80\n"
    "ds_read_b128 v[36:39], v4 offset:96\n"
    "s_waitcnt lgkmcnt(0)\n"
    "v_max3_f32 v28, v32, v33, v34\n"
    "v_max_f32 v28, v28, v35\n"
    "v_pk_add_f32 v[36:37], v[36:37], v[38:39]\n"
    "v_add_f32 v29, v36, v37\n"
    "v_and_or_b32 v28, v28, s78, v30\n"
    "v_and_or_b32 v29, v29, s78, v31\n"
    "s_mov_b64 exec, 0xff\n"
    "buffer_store_dwordx2 v[28:29], v15, s[8:11], s73 offen sc1\n"
    "s_mov_b64 exec, -1\n"
    ".Lw_13:\n"
    "ds_read_b32 v8, v4 offset:24\n"
    "s_waitcnt lgkmcnt(0)\n"
    "v_readfirstlane_b32 s56, v8\n"
    "s_cmp_ge_u32 s56, 4\n"
    "s_cbranch_scc1 .Ld_14\n"
    "s_sleep 1\n"
    "s_sub_u32 s49, s49, 1\n"
    "s_cbranch_scc0 .Lw_13\n"
    "s_mov_b32 s49, 0\n"
    ".Ld_14:\n"
    ".Lp_15:\n"
    "buffer_load_dwordx2 v[44:45], v15, s[8:11], s72 offen offset:0 sc1\n"
    "s_waitcnt vmcnt(0)\n"
    "v_xor_b32 v8, s47, v44\n"
    "v_bitop3_b32 v8, v8, v45, s48 bitop3:0xf6\n"
    "v_and_b32 v8, 0xfff, v8\n"
    "v_cmp_ne_u32 vcc, 0, v8\n"
    "s_cmp_eq_u64 vcc, 0\n"
    "s_cbranch_scc1 .Lpd_16\n"
    "s_sub_u32 s49, s49, 1\n"
    "s_cbranch_scc0 .Lp_15\n"
    "s_mov_b32 s49, 0\n"
    ".Lpd_16:\n"
    "ds_write_b32 v4, v7 offset:68\n"
    "v_and_b32 v44, s78, v44\n"
    "v_and_b32 v45, s78, v45\n"
    "v_mov_b32 v48, v44\n"
    "s_nop 1\n"
    "v_max_f32_dpp v48, v48, v48 row_ror:8 row_mask:0xf bank_mask:0xf bound_ctrl:1\n"
    "v_mov_b32 v49, v48\n"
    "s_nop 1\n"
    "v_permlane16_swap_b32 v48, v49\n"
    "v_max_f32 v48, v48, v49\n"
    "v_mov_b32 v49, v48\n"
    "s_nop 1\n"
    "v_permlane32_swap_b32 v48, v49\n"
    "v_max_f32 v48, v48, v49\n"
    "v_sub_f32 v48, v44, v48\n"
    "v_mul_f32 v48, 0x3fb8aa3b, v48\n"
    "v_exp_f32 v48, v48\n"
    "s_nop 0\n"
    "v_mul_f32 v50, v48, v45\n"
    "s_nop 1\n"
    "v_add_f32_dpp v50, v50, v50 row_ror:8 row_mask:0xf bank_mask:0xf bound_ctrl:1\n"
    "v_mov_b32 v49, v50\n"
    "s_nop 1\n"
    "v_permlane16_swap_b32 v50, v49\n"
    "v_add_f32 v50, v50, v49\n"
    "v_mov_b32 v49, v50\n"
    "s_nop 1\n"
    "v_permlane32_swap_b32 v50, v49\n"
    "v_add_f32 v50, v50, v49\n"
    "v_rcp_f32 v50, v50\n"
    "s_nop 0\n"
    "v_mul_f32 v18, v48, v50\n"
    "v_mov_b32 v19, v18\n"
    "; ---- gather 48 dims of o from the eight splits, normalise, round to bf16, hand to the W waves\n"
    ".Lw_17:\n"
    "ds_read_b32 v8, v4 offset:40\n"
    "s_waitcnt lgkmcnt(0)\n"
    "v_readfirstlane_b32 s56, v8\n"
    "s_cmp_ge_u32 s56, 3\n"
    "s_cbranch_scc1 .Ld_18\n"
    "s_sleep 1\n"
    "s_sub_u32 s49, s49, 1\n"
    "s_cbranch_scc0 .Lw_17\n"
    "s_mov_b32 s49, 0\n"
    ".Ld_18:\n"
    "s_sleep 6\n"
    "s_add_u32 s76, s3, 16025\n"
    "s_swappc_b64 vcc, s[74:75]\n"
    ".Lp_19:\n"
    "buffer_load_dwordx2 v[20:21], v16, s[8:11], s70 offen offset:0 sc1\n"
    "buffer_load_dwordx2 v[22:23], v16, s[8:11], s70 offen offset:64 sc1\n"
    "buffer_load_dwordx2 v[24:25], v16, s[8:11], s70 offen offset:128 sc1\n"
    "s_waitcnt vmcnt(2)\n"
    "v_xor_b32 v8, s47, v20\n"
    "v_bitop3_b32 v8, v8, v21, s48 bitop3:0xf6\n"
    "s_waitcnt vmcnt(1)\n"
    "v_bitop3_b32 v8, v8, v22, s47 bitop3:0xf6\n"
    "v_bitop3_b32 v8, v8, v23, s48 bitop3:0xf6\n"
    "s_waitcnt vmcnt(0)\n"
    "v_bitop3_b32 v8, v8, v24, s47 bitop3:0xf6\n"
    "v_bitop3_b32 v8, v8, v25, s48 bitop3:0xf6\n"
    "v_and_b32 v8, 0xfff, v8\n"
    "v_cmp_ne_u32 vcc, 0, v8\n"
    "s_cmp_eq_u64 vcc, 0\n"
    "s_cbranch_scc1 .Lpd_20\n"
    "s_sub_u32 s49, s49, 1\n"
    "s_cbranch_scc0 .Lp_19\n"
    "s_mov_b32 s49, 0\n"
    ".Lpd_20:\n"
    "v_and_b32 v20, s78, v20\n"
    "v_and_b32 v21, s78, v21\n"
    "v_and_b32 v22, s78, v22\n"
    "v_and_b32 v23, s78, v23\n"
    "v_and_b32 v24, s78, v24\n"
    "v_and_b32 v25, s78, v25\n"
    "v_pk_mul_f32 v[20:21], v[20:21], v[18:19]\n"
    "v_pk_mul_f32 v[22:23], v[22:23], v[18:19]\n"
    "v_pk_mul_f32 v[24:25], v[24:25], v[18:19]\n"
    "s_nop 1\n"
    "v_permlane32_swap_b32 v20, v24\n"
    "v_permlane32_swap_b32 v21, v25\n"
    "v_permlane32_swap_b32 v22, v26\n"
    "v_permlane32_swap_b32 v23, v27\n"
    "v_pk_add_f32 v[20:21], v[20:21], v[24:25]\n"
    "v_pk_add_f32 v[22:23], v[22:23], v[26:27]\n"
    "s_nop 1\n"
    "v_permlane16_swap_b32 v20, v22\n"
    "v_permlane16_swap_b32 v21, v23\n"
    "v_pk_add_f32 v[20:21], v[20:21], v[22:23]\n"
    "s_nop 1\n"
    "v_add_f32_dpp v8, v20, v20 row_ror:8 row_mask:0xf bank_mask:0xf bound_ctrl:1\n"
    "v_add_f32_dpp v9, v21, v21 row_ror:8 row_mask:0xf bank_mask:0xf bound_ctrl:1\n"
    "v_cvt_pk_bf16_f32 v8, v8, v9\n"
    "s_mov_b32 exec_hi, 0xffff\n"
    "ds_write_b32 v17, v8 offset:128\n"
    "s_mov_b64 exec, 0xff\n"
    "ds_write_b32 v5, v7 offset:140\n"
    "s_endpgm\n"
    "; ==== K waves: scores = bf16(bf16(q.k) * Dh^-0.5), true max over the workgroup, p = exp(score - max) ====\n"
    ".Lk_main:\n"
    "s_setprio 0\n"
    "s_mov_b32 s60, 0x05040100\n"
    "s_mov_b32 s61, 0x07060302\n"
    "s_mov_b32 s62, 0xffff0000\n"
    "s_mov_b32 s64, 0x00ff00ff\n"
    "s_mov_b32 s65, 0x00ff00ff\n"
    "s_mov_b32 s78, 0xfffff000\n"
    "v_lshrrev_b32 v3, 4, v0\n"
    "v_lshl_or_b32 v3, v3, 3, v2\n"
    "v_lshlrev_b32 v3, 3, v3 ; pos*8\n"
    "v_mov_b32 v24, 0\n"
    "v_mov_b32 v25, 0\n"
    "v_mov_b32 v26, 0\n"
    "v_mov_b32 v27, 0\n"
    "v_mov_b32 v28, 0\n"
    "v_mov_b32 v29, 0\n"
    "v_mov_b32 v30, 0\n"
    "v_mov_b32 v31, 0\n"
    ".Lw_21:\n"
    "ds_read_b32 v8, v4 offset:8\n"
    "s_waitcnt lgkmcnt(0)\n"
    "v_readfirstlane_b32 s56, v8\n"
    "s_cmp_ge_u32 s56, 1\n"
    "s_cbranch_scc1 .Ld_22\n"
    "s_sleep 1\n"
    "s_sub_u32 s49, s49, 1\n"
    "s_cbranch_scc0 .Lw_21\n"
    "s_mov_b32 s49, 0\n"
    ".Ld_22:\n"
    "ds_read_b128 v[12:15], v5 offset:512\n"
    "ds_read_b128 v[16:19], v5 offset:640\n"
    "ds_read_b128 v[20:23], v5 offset:768\n"
    "ds_read_b32 v8, v4 offset:64\n"
    "s_waitcnt lgkmcnt(0)\n"
    "v_readfirstlane_b32 s79, v8\n"
    "s_and_b32 s47, s79, 0xfff\n"
    "s_xor_b32 s48, s47, 0xa53\n"
    "s_waitcnt vmcnt(23)\n"
    "v_dot2c_f32_bf16 v24, v32, v12\n"
    "v_dot2c_f32_bf16 v24, v33, v13\n"
    "v_dot2c_f32_bf16 v24, v34, v14\n"
    "v_dot2c_f32_bf16 v24, v35, v15\n"
    "s_waitcnt vmcnt(22)\n"
    "v_dot2c_f32_bf16 v24, v36, v16\n"
    "v_dot2c_f32_bf16 v24, v37, v17\n"
    "v_dot2c_f32_bf16 v24, v38, v18\n"
    "v_dot2c_f32_bf16 v24, v39, v19\n"
    "s_waitcnt vmcnt(21)\n"
    "v_dot2c_f32_bf16 v24, v40, v20\n"
    "v_dot2c_f32_bf16 v24, v41, v21\n"
    "v_dot2c_f32_bf16 v24, v42, v22\n"
    "v_dot2c_f32_bf16 v24, v43, v23\n"
    "s_waitcnt vmcnt(20)\n"
    "v_dot2c_f32_bf16 v25, v44, v12\n"
    "v_dot2c_f32_bf16 v25, v45, v13\n"
    "v_dot2c_f32_bf16 v25, v46, v14\n"
    "v_dot2c_f32_bf16 v25, v47, v15\n"
    "s_waitcnt vmcnt(19)\n"
    "v_dot2c_f32_bf16 v25, v48, v16\n"
    "v_dot2c_f32_bf16 v25, v49, v17\n"
    "v_dot2c_f32_bf16 v25, v50, v18\n"
    "v_dot2c_f32_bf16 v25, v51, v19\n"
    "s_waitcnt vmcnt(18)\n"
    "v_dot2c_f32_bf16 v25, v52, v20\n"
    "v_dot2c_f32_bf16 v25, v53, v21\n"
    "v_dot2c_f32_bf16 v25, v54, v22\n"
    "v_dot2c_f32_bf16 v25, v55, v23\n"
    "s_waitcnt vmcnt(17)\n"
    "v_dot2c_f32_bf16 v26, v56, v12\n"
    "v_dot2c_f32_bf16 v26, v57, v13\n"
    "v_dot2c_f32_bf16 v26, v58, v14\n"
    "v_dot2c_f32_bf16 v26, v59, v15\n"
    "s_waitcnt vmcnt(16)\n"
    "v_dot2c_f32_bf16 v26, v60, v16\n"
    "v_dot2c_f32_bf16 v26, v61, v17\n"
    "v_dot2c_f32_bf16 v26, v62, v18\n"
    "v_dot2c_f32_bf16 v26, v63, v19\n"
    "s_waitcnt vmcnt(15)\n"
    "v_dot2c_f32_bf16 v26, v64, v20\n"
    "v_dot2c_f32_bf16 v26, v65, v21\n"
    "v_dot2c_f32_bf16 v26, v66, v22\n"
    "v_dot2c_f32_bf16 v26, v67, v23\n"
    "s_waitcnt vmcnt(14)\n"
    "v_dot2c_f32_bf16 v27, v68, v12\n"
    "v_dot2c_f32_bf16 v27, v69, v13\n"
    "v_dot2c_f32_bf16 v27, v70, v14\n"
    "v_dot2c_f32_bf16 v27, v71, v15\n"
    "s_waitcnt vmcnt(13)\n"
    "v_dot2c_f32_bf16 v27, v72, v16\n"
    "v_dot2c_f32_bf16 v27, v73, v17\n"
    "v_dot2c_f32_bf16 v27, v74, v18\n"
    "v_dot2c_f32_bf16 v27, v75, v19\n"
    "s_waitcnt vmcnt(12)\n"
    "v_dot2c_f32_bf16 v27, v76, v20\n"
    "v_dot2c_f32_bf16 v27, v77, v21\n"
    "v_dot2c_f32_bf16 v27, v78, v22\n"
    "v_dot2c_f32_bf16 v27, v79, v23\n"
    "s_waitcnt vmcnt(11)\n"
    "v_dot2c_f32_bf16 v28, v80, v12\n"
    "v_dot2c_f32_bf16 v28, v81, v13\n"
    "v_dot2c_f32_bf16 v28, v82, v14\n"
    "v_dot2c_f32_bf16 v28, v83, v15\n"
    "s_waitcnt vmcnt(10)\n"
    "v_dot2c_f32_bf16 v28, v84, v16\n"
    "v_dot2c_f32_bf16 v28, v85, v17\n"
    "v_dot2c_f32_bf16 v28, v86, v18\n"
    "v_dot2c_f32_bf16 v28, v87, v19\n"
    "s_waitcnt vmcnt(9)\n"
    "v_dot2c_f32_bf16 v28, v88, v20\n"
    "v_dot2c_f32_bf16 v28, v89, v21\n"
    "v_dot2c_f32_bf16 v28, v90, v22\n"
    "v_dot2c_f32_bf16 v28, v91, v23\n"
    "s_waitcnt vmcnt(8)\n"
    "v_dot2c_f32_bf16 v29, v92, v12\n"
    "v_dot2c_f32_bf16 v29, v93, v13\n"
    "v_dot2c_f32_bf16 v29, v94, v14\n"
    "v_dot2c_f32_bf16 v29, v95, v15\n"
    "s_waitcnt vmcnt(7)\n"
    "v_dot2c_f32_bf16 v29, v96, v16\n"
    "v_dot2c_f32_bf16 v29, v97, v17\n"
    "v_dot2c_f32_bf16 v29, v98, v18\n"
    "v_dot2c_f32_bf16 v29, v99, v19\n"
    "s_waitcnt vmcnt(6)\n"
    "v_dot2c_f32_bf16 v29, v100, v20\n"
    "v_dot2c_f32_bf16 v29, v101, v21\n"
    "v_dot2c_f32_bf16 v29, v102, v22\n"
    "v_dot2c_f32_bf16 v29, v103, v23\n"
    "s_waitcnt vmcnt(5)\n"
    "v_dot2c_f32_bf16 v30, v104, v12\n"
    "v_dot2c_f32_bf16 v30, v105, v13\n"
    "v_dot2c_f32_bf16 v30, v106, v14\n"
    "v_dot2c_f32_bf16 v30, v107, v15\n"
    "s_waitcnt vmcnt(4)\n"
    "v_dot2c_f32_bf16 v30, v108, v16\n"
    "v_dot2c_f32_bf16 v30, v109, v17\n"
    "v_dot2c_f32_bf16 v30, v110, v18\n"
    "v_dot2c_f32_bf16 v30, v111, v19\n"
    "s_waitcnt vmcnt(3)\n"
    "v_dot2c_f32_bf16 v30, v112, v20\n"
    "v_dot2c_f32_bf16 v30, v113, v21\n"
    "v_dot2c_f32_bf16 v30, v114, v22\n"
    "v_dot2c_f32_bf16 v30, v115, v23\n"
    "s_waitcnt vmcnt(2)\n"
    "v_dot2c_f32_bf16 v31, v116, v12\n"
    "v_dot2c_f32_bf16 v31, v117, v13\n"
    "v_dot2c_f32_bf16 v31, v118, v14\n"
    "v_dot2c_f32_bf16 v31, v119, v15\n"
    "s_waitcnt vmcnt(1)\n"
    "v_dot2c_f32_bf16 v31, v120, v16\n"
    "v_dot2c_f32_bf16 v31, v121, v17\n"
    "v_dot2c_f32_bf16 v31, v122, v18\n"
    "v_dot2c_f32_bf16 v31, v123, v19\n"
    "s_waitcnt vmcnt(0)\n"
    "v_dot2c_f32_bf16 v31, v124, v20\n"
    "v_dot2c_f32_bf16 v31, v125, v21\n"
    "v_dot2c_f32_bf16 v31, v126, v22\n"
    "v_dot2c_f32_bf16 v31, v127, v23\n"
    "v_add_f32_dpp v24, v24, v24 row_half_mirror row_mask:0xf bank_mask:0x5\n"
    "v_add_f32_dpp v25, v25, v25 row_half_mirror row_mask:0xf bank_mask:0x5\n"
    "v_add_f32_dpp v26, v26, v26 row_half_mirror row_mask:0xf bank_mask:0x5\n"
    "v_add_f32_dpp v27, v27, v27 row_half_mirror row_mask:0xf bank_mask:0x5\n"
    "v_add_f32_dpp v24, v28, v28 row_half_mirror row_mask:0xf bank_mask:0xa\n"
    "v_add_f32_dpp v25, v29, v29 row_half_mirror row_mask:0xf bank_mask:0xa\n"
    "v_add_f32_dpp v26, v30, v30 row_half_mirror row_mask:0xf bank_mask:0xa\n"
    "v_add_f32_dpp v27, v31, v31 row_half_mirror row_mask:0xf bank_mask:0xa\n"
    "v_add_f32_dpp v24, v24, v24 quad_perm:[1,0,3,2] row_mask:0xf bank_mask:0xf bound_ctrl:1\n"
    "v_add_f32_dpp v25, v25, v25 quad_perm:[1,0,3,2] row_mask:0xf bank_mask:0xf bound_ctrl:1\n"
    "v_add_f32_dpp v26, v26, v26 quad_perm:[1,0,3,2] row_mask:0xf bank_mask:0xf bound_ctrl:1\n"
    "v_add_f32_dpp v27, v27, v27 quad_perm:[1,0,3,2] row_mask:0xf bank_mask:0xf bound_ctrl:1\n"
    "v_add_f32_dpp v24, v24, v24 quad_perm:[2,3,0,1] row_mask:0xf bank_mask:0xf bound_ctrl:1\n"
    "v_add_f32_dpp v25, v25, v25 quad_perm:[2,3,0,1] row_mask:0xf bank_mask:0xf bound_ctrl:1\n"
    "v_add_f32_dpp v26, v26, v26 quad_perm:[2,3,0,1] row_mask:0xf bank_mask:0xf bound_ctrl:1\n"
    "v_add_f32_dpp v27, v27, v27 quad_perm:[2,3,0,1] row_mask:0xf bank_mask:0xf bound_ctrl:1\n"
    "s_mov_b32 s66, 0x3d93cd3a\n"
    "s_mov_b32 s67, 0x3fb8aa3b\n"
    "v_cvt_pk_bf16_f32 v8, v24, v25\n"
    "v_lshlrev_b32 v24, 16, v8\n"
    "v_and_b32 v25, s62, v8\n"
    "v_mul_f32 v24, s66, v24\n"
    "v_mul_f32 v25, s66, v25\n"
    "v_cvt_pk_bf16_f32 v8, v24, v25\n"
    "v_lshlrev_b32 v24, 16, v8\n"
    "v_and_b32 v25, s62, v8\n"
    "v_cvt_pk_bf16_f32 v8, v26, v27\n"
    "v_lshlrev_b32 v26, 16, v8\n"
    "v_and_b32 v27, s62, v8\n"
    "v_mul_f32 v26, s66, v26\n"
    "v_mul_f32 v27, s66, v27\n"
    "v_cvt_pk_bf16_f32 v8, v26, v27\n"
    "v_lshlrev_b32 v26, 16, v8\n"
    "v_and_b32 v27, s62, v8\n"
    "v_max3_f32 v28, v24, v25, v26\n"
    "v_max_f32 v28, v28, v27\n"
    "s_nop 1\n"
    "v_max_f32_dpp v28, v28, v28 row_half_mirror row_mask:0xf bank_mask:0xf bound_ctrl:1\n"
    "s_nop 1\n"
    "v_max_f32_dpp v28, v28, v28 row_ror:8 row_mask:0xf bank_mask:0xf bound_ctrl:1\n"
    "v_mov_b32 v29, v28\n"
    "s_nop 1\n"
    "v_permlane16_swap_b32 v28, v29\n"
    "v_max_f32 v28, v28, v29\n"
    "v_mov_b32 v29, v28\n"
    "s_nop 1\n"
    "v_permlane32_swap_b32 v28, v29\n"
    "v_max_f32 v28, v28, v29\n"
    "s_lshl_b32 s57, s63, 2\n"
    "v_mov_b32 v10, s57\n"
    "s_mov_b64 exec, 1\n"
    "ds_write_b32 v10, v28 offset:80\n"
    "ds_add_u32 v4, v7 offset:16\n"
    "s_mov_b64 exec, -1\n"
    "s_lshr_b32 s57, s63, 1\n"
    "s_lshl_b32 s57, s57, 8\n"
    "s_and_b32 s58, s63, 1\n"
    "s_lshl4_add_u32 s57, s58, s57\n"
    "v_lshl_add_u32 v11, v1, 5, s57\n"
    "v_and_b32 v9, 4, v0\n"
    "v_lshl_add_u32 v11, v9, 1, v11 ; P address: half*256 + r*32 + (kw&1)*16 + hq*8\n"
    ".Lw_23:\n"
    "ds_read_b32 v8, v4 offset:16\n"
    "s_waitcnt lgkmcnt(0)\n"
    "v_readfirstlane_b32 s56, v8\n"
    "s_cmp_ge_u32 s56, 4\n"
    "s_cbranch_scc1 .Ld_24\n"
    "s_sub_u32 s49, s49, 1\n"
    "s_cbranch_scc0 .Lw_23\n"
    "s_mov_b32 s49, 0\n"
    ".Ld_24:\n"
    "ds_read_b128 v[28:31], v4 offset:80\n"
    "s_waitcnt lgkmcnt(0)\n"
    "v_max3_f32 v28, v28, v29, v30\n"
    "v_max_f32 v28, v28, v31\n"
    "v_sub_f32 v24, v24, v28\n"
    "v_sub_f32 v25, v25, v28\n"
    "v_sub_f32 v26, v26, v28\n"
    "v_sub_f32 v27, v27, v28\n"
    "v_mul_f32 v24, s67, v24\n"
    "v_mul_f32 v25, s67, v25\n"
    "v_mul_f32 v26, s67, v26\n"
    "v_mul_f32 v27, s67, v27\n"
    "v_exp_f32 v24, v24\n"
    "v_exp_f32 v25, v25\n"
    "v_exp_f32 v26, v26\n"
    "v_exp_f32 v27, v27\n"
    "v_cvt_pk_bf16_f32 v28, v24, v25\n"
    "v_cvt_pk_bf16_f32 v29, v26, v27\n"
    "ds_write_b64 v11, v[28:29] offset:1024\n"
    "v_pk_add_f32 v[24:25], v[24:25], v[26:27]\n"
    "v_add_f32 v24, v24, v25\n"
    "s_nop 1\n"
    "v_add_f32_dpp v24, v24, v24 row_half_mirror row_mask:0xf bank_mask:0xf bound_ctrl:1\n"
    "s_nop 1\n"
    "v_add_f32_dpp v24, v24, v24 row_ror:8 row_mask:0xf bank_mask:0xf bound_ctrl:1\n"
    "v_mov_b32 v25, v24\n"
    "s_nop 1\n"
    "v_permlane16_swap_b32 v24, v25\n"
    "v_add_f32 v24, v24, v25\n"
    "v_mov_b32 v25, v24\n"
    "s_nop 1\n"
    "v_permlane32_swap_b32 v24, v25\n"
    "v_add_f32 v24, v24, v25\n"
    "s_mov_b64 exec, 1\n"
    "ds_write_b32 v10, v24 offset:96\n"
    "ds_add_u32 v4, v7 offset:20\n"
    "s_mov_b64 exec, -1\n"
    "s_branch .Lfinal\n"
    "; ---- V waves, early half: leave the partial in LDS\n"
    ".Lv_early:\n"
    "s_waitcnt vmcnt(0)\n"
    "v_perm_b32 v8, v124, v120, s60\n"
    "v_dot2c_f32_bf16 v20, v8, v19\n"
    "v_perm_b32 v9, v124, v120, s61\n"
    "v_dot2c_f32_bf16 v21, v9, v19\n"
    "v_perm_b32 v8, v125, v121, s60\n"
    "v_dot2c_f32_bf16 v22, v8, v19\n"
    "v_perm_b32 v9, v125, v121, s61\n"
    "v_dot2c_f32_bf16 v23, v9, v19\n"
    "v_perm_b32 v8, v126, v122, s60\n"
    "v_dot2c_f32_bf16 v24, v8, v19\n"
    "v_perm_b32 v9, v126, v122, s61\n"
    "v_dot2c_f32_bf16 v25, v9, v19\n"
    "v_perm_b32 v8, v127, v123, s60\n"
    "v_dot2c_f32_bf16 v26, v8, v19\n"
    "v_perm_b32 v9, v127, v123, s61\n"
    "v_dot2c_f32_bf16 v27, v9, v19\n"
    "v_permlane32_swap_b32 v20, v24\n"
    "v_permlane32_swap_b32 v21, v25\n"
    "v_permlane32_swap_b32 v22, v26\n"
    "v_permlane32_swap_b32 v23, v27\n"
    "v_pk_add_f32 v[20:21], v[20:21], v[24:25]\n"
    "v_pk_add_f32 v[22:23], v[22:23], v[26:27]\n"
    "s_nop 1\n"
    "v_permlane16_swap_b32 v20, v22\n"
    "v_permlane16_swap_b32 v21, v23\n"
    "v_pk_add_f32 v[20:21], v[20:21], v[22:23]\n"
    "s_nop 1\n"
    "v_add_f32_dpp v28, v20, v20 row_ror:8 row_mask:0xf bank_mask:0xf bound_ctrl:1\n"
    "v_add_f32_dpp v29, v21, v21 row_ror:8 row_mask:0xf bank_mask:0xf bound_ctrl:1\n"
    "ds_write_b64 v11, v[28:29] offset:1536\n"
    "s_mov_b64 exec, 1\n"
    "ds_add_u32 v34, v7 offset:28\n"
    "s_mov_b64 exec, -1\n"
    "s_add_u32 s63, s63, 4\n"
    "s_branch .Lfinal\n"
    ".p2align 12\n"
    "; ==== W waves: first-phase GEMV, down projection, output projection ====\n"
    ".Lw_main:\n"
    "s_setprio 3\n"
    "s_mov_b32 s60, 0x05040100\n"
    "s_mov_b32 s61, 0x07060302\n"
    "s_mov_b32 s62, 0xffff0000\n"
    "s_mov_b32 s64, 0x00ff00ff\n"
    "s_mov_b32 s65, 0x00ff00ff\n"
    "s_mov_b32 s78, 0xfffff000\n"
    "v_lshrrev_b32 v3, 4, v0\n"
    "v_lshl_or_b32 v3, v3, 3, v2\n"
    "v_lshlrev_b32 v3, 3, v3 ; pos*8\n"
    "s_mov_b32 s70, 0\n"
    ".Lw_25:\n"
    "ds_read_b32 v8, v4 offset:0\n"
    "s_waitcnt lgkmcnt(0)\n"
    "v_readfirstlane_b32 s56, v8\n"
    "s_cmp_ge_u32 s56, 1\n"
    "s_cbranch_scc1 .Ld_26\n"
    "s_sleep 1\n"
    "s_sub_u32 s49, s49, 1\n"
    "s_cbranch_scc0 .Lw_25\n"
    "s_mov_b32 s49, 0\n"
    ".Ld_26:\n"
    "ds_read_b32 v8, v4 offset:64\n"
    "s_waitcnt lgkmcnt(0)\n"
    "v_readfirstlane_b32 s79, v8\n"
    "s_and_b32 s47, s79, 0xfff\n"
    "s_xor_b32 s48, s47, 0xa53\n"
    "v_mov_b32 v30, s47\n"
    "v_mov_b32 v31, s48\n"
    ".Lw_again:\n"
    "s_lshl_b32 s56, s66, 1\n"
    "s_add_u32 s56, s56, 2304\n"
    "v_mul_u32_u24 v10, 20, v1\n"
    "v_add_u32 v10, s56, v10\n"
    "ds_read2_b32 v[12:13], v10 offset0:0 offset1:1\n"
    "ds_read2_b32 v[14:15], v10 offset0:2 offset1:3\n"
    "ds_read_b32 v16, v10 offset:16\n"
    "v_mov_b32 v20, 0\n"
    "v_mov_b32 v21, 0\n"
    "v_mov_b32 v22, 0\n"
    "v_mov_b32 v23, 0\n"
    "v_mov_b32 v24, 0\n"
    "v_mov_b32 v25, 0\n"
    "v_mov_b32 v26, 0\n"
    "v_mov_b32 v27, 0\n"
    "s_waitcnt lgkmcnt(0)\n"
    "s_waitcnt vmcnt(8)\n"
    "v_perm_b32 v8, v68, v64, s60\n"
    "v_dot2c_f32_bf16 v20, v8, v12\n"
    "v_perm_b32 v9, v68, v64, s61\n"
    "v_dot2c_f32_bf16 v21, v9, v12\n"
    "v_perm_b32 v8, v69, v65, s60\n"
    "v_dot2c_f32_bf16 v22, v8, v12\n"
    "v_perm_b32 v9, v69, v65, s61\n"
    "v_dot2c_f32_bf16 v23, v9, v12\n"
    "v_perm_b32 v8, v70, v66, s60\n"
    "v_dot2c_f32_bf16 v24, v8, v12\n"
    "v_perm_b32 v9, v70, v66, s61\n"
    "v_dot2c_f32_bf16 v25, v9, v12\n"
    "v_perm_b32 v8, v71, v67, s60\n"
    "v_dot2c_f32_bf16 v26, v8, v12\n"
    "v_perm_b32 v9, v71, v67, s61\n"
    "v_dot2c_f32_bf16 v27, v9, v12\n"
    "s_waitcnt vmcnt(6)\n"
    "v_perm_b32 v8, v76, v72, s60\n"
    "v_dot2c_f32_bf16 v20, v8, v13\n"
    "v_perm_b32 v9, v76, v72, s61\n"
    "v_dot2c_f32_bf16 v21, v9, v13\n"
    "v_perm_b32 v8, v77, v73, s60\n"
    "v_dot2c_f32_bf16 v22, v8, v13\n"
    "v_perm_b32 v9, v77, v73, s61\n"
    "v_dot2c_f32_bf16 v23, v9, v13\n"
    "v_perm_b32 v8, v78, v74, s60\n"
    "v_dot2c_f32_bf16 v24, v8, v13\n"
    "v_perm_b32 v9, v78, v74, s61\n"
    "v_dot2c_f32_bf16 v25, v9, v13\n"
    "v_perm_b32 v8, v79, v75, s60\n"
    "v_dot2c_f32_bf16 v26, v8, v13\n"
    "v_perm_b32 v9, v79, v75, s61\n"
    "v_dot2c_f32_bf16 v27, v9, v13\n"
    "s_waitcnt vmcnt(4)\n"
    "v_perm_b32 v8, v84, v80, s60\n"
    "v_dot2c_f32_bf16 v20, v8, v14\n"
    "v_perm_b32 v9, v84, v80, s61\n"
    "v_dot2c_f32_bf16 v21, v9, v14\n"
    "v_perm_b32 v8, v85, v81, s60\n"
    "v_dot2c_f32_bf16 v22, v8, v14\n"
    "v_perm_b32 v9, v85, v81, s61\n"
    "v_dot2c_f32_bf16 v23, v9, v14\n"
    "v_perm_b32 v8, v86, v82, s60\n"
    "v_dot2c_f32_bf16 v24, v8, v14\n"
    "v_perm_b32 v9, v86, v82, s61\n"
    "v_dot2c_f32_bf16 v25, v9, v14\n"
    "v_perm_b32 v8, v87, v83, s60\n"
    "v_dot2c_f32_bf16 v26, v8, v14\n"
    "v_perm_b32 v9, v87, v83, s61\n"
    "v_dot2c_f32_bf16 v27, v9, v14\n"
    "s_waitcnt vmcnt(2)\n"
    "v_perm_b32 v8, v92, v88, s60\n"
    "v_dot2c_f32_bf16 v20, v8, v15\n"
    "v_perm_b32 v9, v92, v88, s61\n"
    "v_dot2c_f32_bf16 v21, v9, v15\n"
    "v_perm_b32 v8, v93, v89, s60\n"
    "v_dot2c_f32_bf16 v22, v8, v15\n"
    "v_perm_b32 v9, v93, v89, s61\n"
    "v_dot2c_f32_bf16 v23, v9, v15\n"
    "v_perm_b32 v8, v94, v90, s60\n"
    "v_dot2c_f32_bf16 v24, v8, v15\n"
    "v_perm_b32 v9, v94, v90, s61\n"
    "v_dot2c_f32_bf16 v25, v9, v15\n"
    "v_perm_b32 v8, v95, v91, s60\n"
    "v_dot2c_f32_bf16 v26, v8, v15\n"
    "v_perm_b32 v9, v95, v91, s61\n"
    "v_dot2c_f32_bf16 v27, v9, v15\n"
    "s_waitcnt vmcnt(0)\n"
    "v_perm_b32 v8, v100, v96, s60\n"
    "v_dot2c_f32_bf16 v20, v8, v16\n"
    "v_perm_b32 v9, v100, v96, s61\n"
    "v_dot2c_f32_bf16 v21, v9, v16\n"
    "v_perm_b32 v8, v101, v97, s60\n"
    "v_dot2c_f32_bf16 v22, v8, v16\n"
    "v_perm_b32 v9, v101, v97, s61\n"
    "v_dot2c_f32_bf16 v23, v9, v16\n"
    "v_perm_b32 v8, v102, v98, s60\n"
    "v_dot2c_f32_bf16 v24, v8, v16\n"
    "v_perm_b32 v9, v102, v98, s61\n"
    "v_dot2c_f32_bf16 v25, v9, v16\n"
    "v_perm_b32 v8, v103, v99, s60\n"
    "v_dot2c_f32_bf16 v26, v8, v16\n"
    "v_perm_b32 v9, v103, v99, s61\n"
    "v_dot2c_f32_bf16 v27, v9, v16\n"
    "v_permlane32_swap_b32 v20, v24\n"
    "v_permlane32_swap_b32 v21, v25\n"
    "v_permlane32_swap_b32 v22, v26\n"
    "v_permlane32_swap_b32 v23, v27\n"
    "v_pk_add_f32 v[20:21], v[20:21], v[24:25]\n"
    "v_pk_add_f32 v[22:23], v[22:23], v[26:27]\n"
    "s_nop 1\n"
    "v_permlane16_swap_b32 v20, v22\n"
    "v_permlane16_swap_b32 v21, v23\n"
    "v_pk_add_f32 v[20:21], v[20:21], v[22:23]\n"
    "s_nop 1\n"
    "v_add_f32_dpp v28, v20, v20 row_ror:8 row_mask:0xf bank_mask:0xf bound_ctrl:1\n"
    "v_add_f32_dpp v29, v21, v21 row_ror:8 row_mask:0xf bank_mask:0xf bound_ctrl:1\n"
    "s_cmp_eq_u32 s68, 0\n"
    "s_cbranch_scc1 .Lw_gu\n"
    "s_lshl_b32 s56, s63, 8\n"
    "v_add_u32 v10, s56, v3\n"
    "ds_write_b64 v10, v[28:29] offset:3584\n"
    "s_mov_b64 exec, 1\n"
    "ds_add_u32 v4, v7 offset:4\n"
    "s_mov_b64 exec, -1\n"
    "s_branch .Lw_p2\n"
    ".Lw_gu:\n"
    "s_and_b32 s56, s69, 15\n"
    "s_mul_i32 s56, s56, 6144\n"
    "s_bfe_u32 s57, s69, 0x50004\n"
    "s_lshl_b32 s57, s57, 8\n"
    "s_add_u32 s56, s56, s57\n"
    "s_add_u32 s56, s56, 135168\n"
    "s_bitcmp0_b32 s69, 19\n"
    "s_cbranch_scc1 .Lw_gst\n"
    "s_mov_b32 s70, s56\n"
    "s_lshr_b32 s69, s69, 10\n"
    "s_and_b32 s66, s69, 7\n"
    "s_mul_i32 s66, s66, 80\n"
    "v_mov_b32 v56, v28\n"
    "v_mov_b32 v57, v29\n"
    "v_mov_b64 v[64:65], v[104:105]\n"
    "v_mov_b64 v[66:67], v[106:107]\n"
    "v_mov_b64 v[68:69], v[108:109]\n"
    "v_mov_b64 v[70:71], v[110:111]\n"
    "v_mov_b64 v[72:73], v[112:113]\n"
    "v_mov_b64 v[74:75], v[114:115]\n"
    "v_mov_b64 v[76:77], v[116:117]\n"
    "v_mov_b64 v[78:79], v[118:119]\n"
    "v_mov_b64 v[80:81], v[120:121]\n"
    "v_mov_b64 v[82:83], v[122:123]\n"
    "v_mov_b64 v[84:85], v[124:125]\n"
    "v_mov_b64 v[86:87], v[126:127]\n"
    "v_mov_b64 v[88:89], v[32:33]\n"
    "v_mov_b64 v[90:91], v[34:35]\n"
    "v_mov_b64 v[92:93], v[36:37]\n"
    "v_mov_b64 v[94:95], v[38:39]\n"
    "v_mov_b64 v[96:97], v[40:41]\n"
    "v_mov_b64 v[98:99], v[42:43]\n"
    "v_mov_b64 v[100:101], v[44:45]\n"
    "v_mov_b64 v[102:103], v[46:47]\n"
    "s_branch .Lw_again\n"
    ".Lw_gst:\n"
    "v_and_or_b32 v28, v28, s78, v30\n"
    "v_and_or_b32 v29, v29, s78, v31\n"
    "s_mov_b64 exec, s[64:65]\n"
    "buffer_store_dwordx2 v[28:29], v3, s[8:11], s56 offen sc1\n"
    "s_mov_b64 exec, -1\n"
    "s_cmp_eq_u32 s70, 0\n"
    "s_cbranch_scc1 .Lw_p2\n"
    "v_mov_b32 v58, v30\n"
    "v_mov_b32 v59, v31\n"
    "v_and_or_b32 v56, v56, s78, v58\n"
    "v_and_or_b32 v57, v57, s78, v59\n"
    "s_mov_b64 exec, s[64:65]\n"
    "buffer_store_dwordx2 v[56:57], v3, s[8:11], s70 offen sc1\n"
    "s_mov_b64 exec, -1\n"
    "s_branch .Lw_p2\n"
    ".Lw_idle2:\n"
    "s_setprio 3\n"
    "s_mov_b32 s60, 0x05040100\n"
    "s_mov_b32 s61, 0x07060302\n"
    "s_mov_b32 s62, 0xffff0000\n"
    "s_mov_b32 s64, 0x00ff00ff\n"
    "s_mov_b32 s65, 0x00ff00ff\n"
    "s_mov_b32 s78, 0xfffff000\n"
    "v_lshrrev_b32 v3, 4, v0\n"
    "v_lshl_or_b32 v3, v3, 3, v2\n"
    "v_lshlrev_b32 v3, 3, v3 ; pos*8\n"
    ".Lw_27:\n"
    "ds_read_b32 v8, v4 offset:0\n"
    "s_waitcnt lgkmcnt(0)\n"
    "v_readfirstlane_b32 s56, v8\n"
    "s_cmp_ge_u32 s56, 1\n"
    "s_cbranch_scc1 .Ld_28\n"
    "s_sleep 1\n"
    "s_sub_u32 s49, s49, 1\n"
    "s_cbranch_scc0 .Lw_27\n"
    "s_mov_b32 s49, 0\n"
    ".Ld_28:\n"
    "ds_read_b32 v8, v4 offset:64\n"
    "s_waitcnt lgkmcnt(0)\n"
    "v_readfirstlane_b32 s79, v8\n"
    "s_and_b32 s47, s79, 0xfff\n"
    "s_xor_b32 s48, s47, 0xa53\n"
    "v_mov_b32 v30, s47\n"
    "v_mov_b32 v31, s48\n"
    ".Lw_p2:\n"
    "s_movk_i32 s58, 10240\n"
    "v_mad_u32_u24 v6, v1, s58, v5 ; r*8*1280 + c*16\n"
    "v_lshlrev_b32 v10, 4, v1 ; r*16\n"
    "s_and_b32 s70, s46, 1\n"
    "s_mul_i32 s71, s70, 5\n"
    "s_add_u32 s71, s71, s63 ; line = (s&1)*5 + w\n"
    "s_mul_i32 s57, s45, 245760\n"
    "s_lshl_b32 s58, s71, 7\n"
    "s_add_u32 s57, s57, s58\n"
    "s_lshr_b32 s73, s46, 1\n"
    "s_mul_i32 s72, s73, 3\n"
    "s_cmp_lt_u32 s46, 6\n"
    "s_cbranch_scc1 .Lw_p3\n"
    "s_waitcnt vmcnt(0)\n"
    "s_cmp_lg_u32 s63, 3\n"
    "s_cbranch_scc1 .Lw_ng\n"
    "s_movk_i32 s58, 6144\n"
    "v_lshlrev_b32 v11, 3, v2\n"
    "v_mad_u32_u24 v11, v1, s58, v11\n"
    "s_mul_i32 s66, s45, 192\n"
    "s_add_u32 s66, s66, 135168\n"
    "s_add_u32 s67, s66, 49152\n"
    "v_lshrrev_b32 v17, 4, v0\n"
    "v_lshl_add_u32 v17, v17, 2, v5\n"
    "v_mov_b32 v94, 0\n"
    "v_mov_b32 v95, 0\n"
    "v_mov_b32 v102, 0\n"
    "v_mov_b32 v103, 0\n"
    "s_add_u32 s76, s3, 9181\n"
    "s_swappc_b64 vcc, s[74:75]\n"
    ".Lp_29:\n"
    "buffer_load_dwordx2 v[88:89], v11, s[8:11], s66 offen offset:0 sc1\n"
    "buffer_load_dwordx2 v[90:91], v11, s[8:11], s66 offen offset:64 sc1\n"
    "buffer_load_dwordx2 v[92:93], v11, s[8:11], s66 offen offset:128 sc1\n"
    "buffer_load_dwordx2 v[96:97], v11, s[8:11], s67 offen offset:0 sc1\n"
    "buffer_load_dwordx2 v[98:99], v11, s[8:11], s67 offen offset:64 sc1\n"
    "buffer_load_dwordx2 v[100:101], v11, s[8:11], s67 offen offset:128 sc1\n"
    "s_waitcnt vmcnt(5)\n"
    "v_xor_b32 v8, s47, v88\n"
    "v_bitop3_b32 v8, v8, v89, s48 bitop3:0xf6\n"
    "s_waitcnt vmcnt(4)\n"
    "v_bitop3_b32 v8, v8, v90, s47 bitop3:0xf6\n"
    "v_bitop3_b32 v8, v8, v91, s48 bitop3:0xf6\n"
    "s_waitcnt vmcnt(3)\n"
    "v_bitop3_b32 v8, v8, v92, s47 bitop3:0xf6\n"
    "v_bitop3_b32 v8, v8, v93, s48 bitop3:0xf6\n"
    "s_waitcnt vmcnt(2)\n"
    "v_bitop3_b32 v8, v8, v96, s47 bitop3:0xf6\n"
    "v_bitop3_b32 v8, v8, v97, s48 bitop3:0xf6\n"
    "s_waitcnt vmcnt(1)\n"
    "v_bitop3_b32 v8, v8, v98, s47 bitop3:0xf6\n"
    "v_bitop3_b32 v8, v8, v99, s48 bitop3:0xf6\n"
    "s_waitcnt vmcnt(0)\n"
    "v_bitop3_b32 v8, v8, v100, s47 bitop3:0xf6\n"
    "v_bitop3_b32 v8, v8, v101, s48 bitop3:0xf6\n"
    "v_and_b32 v8, 0xfff, v8\n"
    "v_cmp_ne_u32 vcc, 0, v8\n"
    "s_cmp_eq_u64 vcc, 0\n"
    "s_cbranch_scc1 .Lpd_30\n"
    "s_sub_u32 s49, s49, 1\n"
    "s_cbranch_scc0 .Lp_29\n"
    "s_mov_b32 s49, 0\n"
    ".Lpd_30:\n"
    "v_and_b32 v88, s78, v88\n"
    "v_and_b32 v89, s78, v89\n"
    "v_and_b32 v90, s78, v90\n"
    "v_and_b32 v91, s78, v91\n"
    "v_and_b32 v92, s78, v92\n"
    "v_and_b32 v93, s78, v93\n"
    "v_and_b32 v96, s78, v96\n"
    "v_and_b32 v97, s78, v97\n"
    "v_and_b32 v98, s78, v98\n"
    "v_and_b32 v99, s78, v99\n"
    "v_and_b32 v100, s78, v100\n"
    "v_and_b32 v101, s78, v101\n"
    "v_permlane32_swap_b32 v88, v92\n"
    "v_permlane32_swap_b32 v89, v93\n"
    "v_permlane32_swap_b32 v90, v94\n"
    "v_permlane32_swap_b32 v91, v95\n"
    "v_pk_add_f32 v[88:89], v[88:89], v[92:93]\n"
    "v_pk_add_f32 v[90:91], v[90:91], v[94:95]\n"
    "s_nop 1\n"
    "v_permlane16_swap_b32 v88, v90\n"
    "v_permlane16_swap_b32 v89, v91\n"
    "v_pk_add_f32 v[88:89], v[88:89], v[90:91]\n"
    "s_nop 1\n"
    "v_add_f32_dpp v12, v88, v88 row_ror:8 row_mask:0xf bank_mask:0xf bound_ctrl:1\n"
    "v_add_f32_dpp v13, v89, v89 row_ror:8 row_mask:0xf bank_mask:0xf bound_ctrl:1\n"
    "v_permlane32_swap_b32 v96, v100\n"
    "v_permlane32_swap_b32 v97, v101\n"
    "v_permlane32_swap_b32 v98, v102\n"
    "v_permlane32_swap_b32 v99, v103\n"
    "v_pk_add_f32 v[96:97], v[96:97], v[100:101]\n"
    "v_pk_add_f32 v[98:99], v[98:99], v[102:103]\n"
    "s_nop 1\n"
    "v_permlane16_swap_b32 v96, v98\n"
    "v_permlane16_swap_b32 v97, v99\n"
    "v_pk_add_f32 v[96:97], v[96:97], v[98:99]\n"
    "s_nop 1\n"
    "v_add_f32_dpp v14, v96, v96 row_ror:8 row_mask:0xf bank_mask:0xf bound_ctrl:1\n"
    "v_add_f32_dpp v15, v97, v97 row_ror:8 row_mask:0xf bank_mask:0xf bound_ctrl:1\n"
    "v_cvt_pk_bf16_f32 v12, v12, v13\n"
    "v_cvt_pk_bf16_f32 v14, v14, v15\n"
    "v_and_b32 v13, s62, v12\n"
    "v_and_b32 v15, s62, v14\n"
    "v_lshlrev_b32 v12, 16, v12\n"
    "v_lshlrev_b32 v14, 16, v14\n"
    "v_pk_mul_f32 v[12:13], v[12:13], v[14:15]\n"
    "v_cvt_pk_bf16_f32 v12, v12, v13\n"
    "s_mov_b32 exec_hi, 0xffff\n"
    "ds_write_b32 v17, v12 offset:256\n"
    "s_mov_b64 exec, 0xff\n"
    "ds_write_b32 v5, v7 offset:268\n"
    "s_mov_b64 exec, -1\n"
    ".Lw_ng:\n"
    "v_mov_b32 v20, 0\n"
    "v_mov_b32 v21, 0\n"
    "v_mov_b32 v22, 0\n"
    "v_mov_b32 v23, 0\n"
    "v_mov_b32 v24, 0\n"
    "v_mov_b32 v25, 0\n"
    "v_mov_b32 v26, 0\n"
    "v_mov_b32 v27, 0\n"
    "s_mul_i32 s70, s45, 2560\n"
    "s_lshl_b32 s58, s71, 8\n"
    "s_add_u32 s70, s70, s58\n"
    "s_add_u32 s70, s70, 430080\n"
    ".Lw_hl:\n"
    "ds_read_b128 v[12:15], v10 offset:256\n"
    "s_waitcnt lgkmcnt(0)\n"
    "v_cmp_eq_u32 vcc, 0, v15\n"
    "s_cmp_eq_u64 vcc, 0\n"
    "s_cbranch_scc1 .Lw_hlk\n"
    "s_sleep 1\n"
    "s_sub_u32 s49, s49, 1\n"
    "s_cbranch_scc0 .Lw_hl\n"
    "s_mov_b32 s49, 0\n"
    ".Lw_hlk:\n"
    "v_perm_b32 v8, v36, v32, s60\n"
    "v_dot2c_f32_bf16 v20, v8, v12\n"
    "v_perm_b32 v9, v36, v32, s61\n"
    "v_dot2c_f32_bf16 v21, v9, v12\n"
    "v_perm_b32 v8, v37, v33, s60\n"
    "v_dot2c_f32_bf16 v22, v8, v12\n"
    "v_perm_b32 v9, v37, v33, s61\n"
    "v_dot2c_f32_bf16 v23, v9, v12\n"
    "v_perm_b32 v8, v38, v34, s60\n"
    "v_dot2c_f32_bf16 v24, v8, v12\n"
    "v_perm_b32 v9, v38, v34, s61\n"
    "v_dot2c_f32_bf16 v25, v9, v12\n"
    "v_perm_b32 v8, v39, v35, s60\n"
    "v_dot2c_f32_bf16 v26, v8, v12\n"
    "v_perm_b32 v9, v39, v35, s61\n"
    "v_dot2c_f32_bf16 v27, v9, v12\n"
    "v_perm_b32 v8, v44, v40, s60\n"
    "v_dot2c_f32_bf16 v20, v8, v13\n"
    "v_perm_b32 v9, v44, v40, s61\n"
    "v_dot2c_f32_bf16 v21, v9, v13\n"
    "v_perm_b32 v8, v45, v41, s60\n"
    "v_dot2c_f32_bf16 v22, v8, v13\n"
    "v_perm_b32 v9, v45, v41, s61\n"
    "v_dot2c_f32_bf16 v23, v9, v13\n"
    "v_perm_b32 v8, v46, v42, s60\n"
    "v_dot2c_f32_bf16 v24, v8, v13\n"
    "v_perm_b32 v9, v46, v42, s61\n"
    "v_dot2c_f32_bf16 v25, v9, v13\n"
    "v_perm_b32 v8, v47, v43, s60\n"
    "v_dot2c_f32_bf16 v26, v8, v13\n"
    "v_perm_b32 v9, v47, v43, s61\n"
    "v_dot2c_f32_bf16 v27, v9, v13\n"
    "v_perm_b32 v8, v52, v48, s60\n"
    "v_dot2c_f32_bf16 v20, v8, v14\n"
    "v_perm_b32 v9, v52, v48, s61\n"
    "v_dot2c_f32_bf16 v21, v9, v14\n"
    "v_perm_b32 v8, v53, v49, s60\n"
    "v_dot2c_f32_bf16 v22, v8, v14\n"
    "v_perm_b32 v9, v53, v49, s61\n"
    "v_dot2c_f32_bf16 v23, v9, v14\n"
    "v_perm_b32 v8, v54, v50, s60\n"
    "v_dot2c_f32_bf16 v24, v8, v14\n"
    "v_perm_b32 v9, v54, v50, s61\n"
    "v_dot2c_f32_bf16 v25, v9, v14\n"
    "v_perm_b32 v8, v55, v51, s60\n"
    "v_dot2c_f32_bf16 v26, v8, v14\n"
    "v_perm_b32 v9, v55, v51, s61\n"
    "v_dot2c_f32_bf16 v27, v9, v14\n"
    "v_permlane32_swap_b32 v20, v24\n"
    "v_permlane32_swap_b32 v21, v25\n"
    "v_permlane32_swap_b32 v22, v26\n"
    "v_permlane32_swap_b32 v23, v27\n"
    "v_pk_add_f32 v[20:21], v[20:21], v[24:25]\n"
    "v_pk_add_f32 v[22:23], v[22:23], v[26:27]\n"
    "s_nop 1\n"
    "v_permlane16_swap_b32 v20, v22\n"
    "v_permlane16_swap_b32 v21, v23\n"
    "v_pk_add_f32 v[20:21], v[20:21], v[22:23]\n"
    "s_nop 1\n"
    "v_add_f32_dpp v56, v20, v20 row_ror:8 row_mask:0xf bank_mask:0xf bound_ctrl:1\n"
    "v_add_f32_dpp v57, v21, v21 row_ror:8 row_mask:0xf bank_mask:0xf bound_ctrl:1\n"
    "v_mov_b32 v58, v30\n"
    "v_mov_b32 v59, v31\n"
    ".Lw_p3:\n"
    "; ---- tile of wo (48 rows x 320 columns), streamed behind V\n"
    ".Lw_31:\n"
    "ds_read_b32 v8, v4 offset:24\n"
    "s_waitcnt lgkmcnt(0)\n"
    "v_readfirstlane_b32 s56, v8\n"
    "s_cmp_ge_u32 s56, 6\n"
    "s_cbranch_scc1 .Ld_32\n"
    "s_sleep 1\n"
    "s_sub_u32 s49, s49, 1\n"
    "s_cbranch_scc0 .Lw_31\n"
    "s_mov_b32 s49, 0\n"
    ".Ld_32:\n"
    "ds_read_b32 v8, v4 offset:72\n"
    "s_waitcnt lgkmcnt(0)\n"
    "v_readfirstlane_b32 s56, v8\n"
    "s_cmp_lg_u32 s56, 0\n"
    "s_cselect_b32 s3, s56, s3\n"
    "s_mul_i32 s76, s63, 94\n"
    "s_add_u32 s76, s76, s3\n"
    "s_add_u32 s76, s76, 14432\n"
    "s_movk_i32 s77, 472\n"
    "s_add_u32 s58, s72, 0\n"
    "s_lshr_b32 s59, s58, 2\n"
    "s_and_b32 s58, s58, 3\n"
    "s_lshl_b32 s59, s59, 5\n"
    "s_add_u32 s58, s58, s59\n"
    "s_mul_i32 s58, s58, 2560\n"
    "s_add_u32 s58, s58, s57\n"
    "s_swappc_b64 vcc, s[74:75]\n"
    "buffer_load_dwordx4 v[64:67], v6, s[20:23], s58 offen nt\n"
    "buffer_load_dwordx4 v[68:71], v6, s[20:23], s58 offen offset:1280 nt\n"
    "s_add_u32 s58, s72, 1\n"
    "s_lshr_b32 s59, s58, 2\n"
    "s_and_b32 s58, s58, 3\n"
    "s_lshl_b32 s59, s59, 5\n"
    "s_add_u32 s58, s58, s59\n"
    "s_mul_i32 s58, s58, 2560\n"
    "s_add_u32 s58, s58, s57\n"
    "s_swappc_b64 vcc, s[74:75]\n"
    "buffer_load_dwordx4 v[72:75], v6, s[20:23], s58 offen nt\n"
    "buffer_load_dwordx4 v[76:79], v6, s[20:23], s58 offen offset:1280 nt\n"
    "s_add_u32 s58, s72, 2\n"
    "s_lshr_b32 s59, s58, 2\n"
    "s_and_b32 s58, s58, 3\n"
    "s_lshl_b32 s59, s59, 5\n"
    "s_add_u32 s58, s58, s59\n"
    "s_mul_i32 s58, s58, 2560\n"
    "s_add_u32 s58, s58, s57\n"
    "s_swappc_b64 vcc, s[74:75]\n"
    "buffer_load_dwordx4 v[80:83], v6, s[20:23], s58 offen nt\n"
    "buffer_load_dwordx4 v[84:87], v6, s[20:23], s58 offen offset:1280 nt\n"
    "s_cmp_lt_u32 s46, 6\n"
    "s_cbranch_scc1 .Lw_nm\n"
    ".Lw_33:\n"
    "ds_read_b32 v8, v4 offset:68\n"
    "s_waitcnt lgkmcnt(0)\n"
    "v_readfirstlane_b32 s56, v8\n"
    "s_cmp_ge_u32 s56, 1\n"
    "s_cbranch_scc1 .Ld_34\n"
    "s_sleep 1\n"
    "s_sub_u32 s49, s49, 1\n"
    "s_cbranch_scc0 .Lw_33\n"
    "s_mov_b32 s49, 0\n"
    ".Ld_34:\n"
    ".Lw_nm:\n"
    "v_mov_b32 v20, 0\n"
    "v_mov_b32 v21, 0\n"
    "v_mov_b32 v22, 0\n"
    "v_mov_b32 v23, 0\n"
    "v_mov_b32 v24, 0\n"
    "v_mov_b32 v25, 0\n"
    "v_mov_b32 v26, 0\n"
    "v_mov_b32 v27, 0\n"
    "v_lshrrev_b32 v17, 4, v0\n"
    "v_lshl_add_u32 v17, v2, 2, v17\n"
    "v_lshlrev_b32 v17, 3, v17\n"
    "s_lshl2_add_u32 s72, s45, s73\n"
    "s_mul_i32 s72, s72, 2560\n"
    "s_lshl_b32 s58, s71, 8\n"
    "s_add_u32 s72, s72, s58\n"
    "s_add_u32 s72, s72, 512000\n"
    "s_waitcnt vmcnt(4)\n"
    "v_perm_b32 v88, v68, v64, s60\n"
    "v_perm_b32 v89, v68, v64, s61\n"
    "v_perm_b32 v90, v69, v65, s60\n"
    "v_perm_b32 v91, v69, v65, s61\n"
    "v_perm_b32 v92, v70, v66, s60\n"
    "v_perm_b32 v93, v70, v66, s61\n"
    "v_perm_b32 v94, v71, v67, s60\n"
    "v_perm_b32 v95, v71, v67, s61\n"
    "s_waitcnt vmcnt(2)\n"
    "v_perm_b32 v96, v76, v72, s60\n"
    "v_perm_b32 v97, v76, v72, s61\n"
    "v_perm_b32 v98, v77, v73, s60\n"
    "v_perm_b32 v99, v77, v73, s61\n"
    "v_perm_b32 v100, v78, v74, s60\n"
    "v_perm_b32 v101, v78, v74, s61\n"
    "v_perm_b32 v102, v79, v75, s60\n"
    "v_perm_b32 v103, v79, v75, s61\n"
    "s_waitcnt vmcnt(0)\n"
    "v_perm_b32 v104, v84, v80, s60\n"
    "v_perm_b32 v105, v84, v80, s61\n"
    "v_perm_b32 v106, v85, v81, s60\n"
    "v_perm_b32 v107, v85, v81, s61\n"
    "v_perm_b32 v108, v86, v82, s60\n"
    "v_perm_b32 v109, v86, v82, s61\n"
    "v_perm_b32 v110, v87, v83, s60\n"
    "v_perm_b32 v111, v87, v83, s61\n"
    "s_cmp_lt_u32 s46, 6\n"
    "s_cbranch_scc1 .Lw_p5\n"
    "v_and_or_b32 v56, v56, s78, v58\n"
    "v_and_or_b32 v57, v57, s78, v59\n"
    "s_mov_b64 exec, s[64:65]\n"
    "buffer_store_dwordx2 v[56:57], v17, s[8:11], s70 offen sc1\n"
    "s_mov_b64 exec, -1\n"
    ".Lw_p5:\n"
    ".Lw_p4:\n"
    ".Lw_ol:\n"
    "ds_read_b128 v[12:15], v10 offset:128\n"
    "s_waitcnt lgkmcnt(0)\n"
    "v_cmp_eq_u32 vcc, 0, v15\n"
    "s_cmp_eq_u64 vcc, 0\n"
    "s_cbranch_scc1 .Lw_ook\n"
    "s_sub_u32 s49, s49, 1\n"
    "s_cbranch_scc0 .Lw_ol\n"
    "s_mov_b32 s49, 0\n"
    ".Lw_ook:\n"
    "v_dot2c_f32_bf16 v20, v88, v12\n"
    "v_dot2c_f32_bf16 v21, v89, v12\n"
    "v_dot2c_f32_bf16 v22, v90, v12\n"
    "v_dot2c_f32_bf16 v23, v91, v12\n"
    "v_dot2c_f32_bf16 v24, v92, v12\n"
    "v_dot2c_f32_bf16 v25, v93, v12\n"
    "v_dot2c_f32_bf16 v26, v94, v12\n"
    "v_dot2c_f32_bf16 v27, v95, v12\n"
    "v_dot2c_f32_bf16 v20, v96, v13\n"
    "v_dot2c_f32_bf16 v21, v97, v13\n"
    "v_dot2c_f32_bf16 v22, v98, v13\n"
    "v_dot2c_f32_bf16 v23, v99, v13\n"
    "v_dot2c_f32_bf16 v24, v100, v13\n"
    "v_dot2c_f32_bf16 v25, v101, v13\n"
    "v_dot2c_f32_bf16 v26, v102, v13\n"
    "v_dot2c_f32_bf16 v27, v103, v13\n"
    "v_dot2c_f32_bf16 v20, v104, v14\n"
    "v_dot2c_f32_bf16 v21, v105, v14\n"
    "v_dot2c_f32_bf16 v22, v106, v14\n"
    "v_dot2c_f32_bf16 v23, v107, v14\n"
    "v_dot2c_f32_bf16 v24, v108, v14\n"
    "v_dot2c_f32_bf16 v25, v109, v14\n"
    "v_dot2c_f32_bf16 v26, v110, v14\n"
    "v_dot2c_f32_bf16 v27, v111, v14\n"
    "v_permlane32_swap_b32 v20, v24\n"
    "v_permlane32_swap_b32 v21, v25\n"
    "v_permlane32_swap_b32 v22, v26\n"
    "v_permlane32_swap_b32 v23, v27\n"
    "v_pk_add_f32 v[20:21], v[20:21], v[24:25]\n"
    "v_pk_add_f32 v[22:23], v[22:23], v[26:27]\n"
    "s_nop 1\n"
    "v_permlane16_swap_b32 v20, v22\n"
    "v_permlane16_swap_b32 v21, v23\n"
    "v_pk_add_f32 v[20:21], v[20:21], v[22:23]\n"
    "s_nop 1\n"
    "v_add_f32_dpp v28, v20, v20 row_ror:8 row_mask:0xf bank_mask:0xf bound_ctrl:1\n"
    "v_add_f32_dpp v29, v21, v21 row_ror:8 row_mask:0xf bank_mask:0xf bound_ctrl:1\n"
    "v_and_or_b32 v28, v28, s78, v30\n"
    "v_and_or_b32 v29, v29, s78, v31\n"
    "s_mov_b64 exec, s[64:65]\n"
    "buffer_store_dwordx2 v[28:29], v17, s[8:11], s72 offen sc1\n"
    "s_mov_b64 exec, -1\n"
    "ds_write_b32 v4, v7 offset:44\n"
    "s_endpgm\n"
    "; ==== last wave: gate = silu(down projection), out = x + s * gate ====\n"
    ".Lmaster:\n"
    "s_lshr_b32 s56, s2, 4\n"
    "s_and_b32 s57, s2, 7\n"
    "s_lshl3_add_u32 s56, s56, s57\n"
    "s_lshl_b32 s56, s56, 1\n"
    "s_bfe_u32 s57, s2, 0x10003\n"
    "s_add_u32 s2, s56, s57\n"
    "s_cmp_ge_u32 s2, 20\n"
    "s_cbranch_scc1 .Lf_end\n"
    "s_movk_i32 s58, 2560\n"
    "v_lshrrev_b32 v9, 3, v0\n"
    "v_and_b32 v13, 7, v0\n"
    "v_lshlrev_b32 v13, 4, v13\n"
    "v_mad_u32_u24 v13, v9, s58, v13\n"
    "s_lshl_b32 s58, s2, 7\n"
    "s_add_u32 s70, s58, 430080\n"
    "s_add_u32 s71, s70, 20480\n"
    "s_add_u32 s72, s71, 20480\n"
    "s_add_u32 s73, s72, 20480\n"
    "s_lshl_b32 s57, s2, 6\n"
    "v_lshrrev_b32 v12, 1, v0\n"
    "v_lshl_add_u32 v12, v12, 1, s57 ; column byte offset\n"
    "buffer_load_ushort v14, v12, s[4:7], 0 offen\n"
    "v_bfe_u32 v9, v0, 1, 2\n"
    "v_lshrrev_b32 v11, 3, v0\n"
    "v_lshl_add_u32 v11, v9, 4, v11\n"
    "v_lshlrev_b32 v11, 2, v11 ; bpermute source lane*4: (j&3)*16 + (j>>2)\n"
    "v_lshlrev_b32 v15, 4, v0 ; F address\n"
    "ds_read_b32 v8, v4 offset:72\n"
    "s_waitcnt lgkmcnt(0)\n"
    "v_readfirstlane_b32 s56, v8\n"
    "s_cmp_lg_u32 s56, 0\n"
    "s_cselect_b32 s3, s56, s3\n"
    "s_add_u32 s76, s3, 17500\n"
    "s_swappc_b64 vcc, s[74:75]\n"
    ".Lp_35:\n"
    "buffer_load_dwordx4 v[16:19], v13, s[8:11], s70 offen offset:0 sc1\n"
    "buffer_load_dwordx4 v[20:23], v13, s[8:11], s71 offen offset:0 sc1\n"
    "buffer_load_dwordx4 v[24:27], v13, s[8:11], s72 offen offset:0 sc1\n"
    "buffer_load_dwordx4 v[28:31], v13, s[8:11], s73 offen offset:0 sc1\n"
    "s_waitcnt vmcnt(3)\n"
    "v_xor_b32 v8, s47, v16\n"
    "v_bitop3_b32 v8, v8, v17, s48 bitop3:0xf6\n"
    "v_bitop3_b32 v8, v8, v18, s47 bitop3:0xf6\n"
    "v_bitop3_b32 v8, v8, v19, s48 bitop3:0xf6\n"
    "s_waitcnt vmcnt(2)\n"
    "v_bitop3_b32 v8, v8, v20, s47 bitop3:0xf6\n"
    "v_bitop3_b32 v8, v8, v21, s48 bitop3:0xf6\n"
    "v_bitop3_b32 v8, v8, v22, s47 bitop3:0xf6\n"
    "v_bitop3_b32 v8, v8, v23, s48 bitop3:0xf6\n"
    "s_waitcnt vmcnt(1)\n"
    "v_bitop3_b32 v8, v8, v24, s47 bitop3:0xf6\n"
    "v_bitop3_b32 v8, v8, v25, s48 bitop3:0xf6\n"
    "v_bitop3_b32 v8, v8, v26, s47 bitop3:0xf6\n"
    "v_bitop3_b32 v8, v8, v27, s48 bitop3:0xf6\n"
    "s_waitcnt vmcnt(0)\n"
    "v_bitop3_b32 v8, v8, v28, s47 bitop3:0xf6\n"
    "v_bitop3_b32 v8, v8, v29, s48 bitop3:0xf6\n"
    "v_bitop3_b32 v8, v8, v30, s47 bitop3:0xf6\n"
    "v_bitop3_b32 v8, v8, v31, s48 bitop3:0xf6\n"
    "v_and_b32 v8, 0xfff, v8\n"
    "v_cmp_ne_u32 vcc, 0, v8\n"
    "s_cmp_eq_u64 vcc, 0\n"
    "s_cbranch_scc1 .Lpd_36\n"
    "s_sleep 2\n"
    "s_sub_u32 s49, s49, 1\n"
    "s_cbranch_scc0 .Lp_35\n"
    "s_mov_b32 s49, 0\n"
    ".Lpd_36:\n"
    "s_waitcnt vmcnt(0)\n"
    "v_and_b32 v16, s78, v16\n"
    "v_and_b32 v17, s78, v17\n"
    "v_and_b32 v18, s78, v18\n"
    "v_and_b32 v19, s78, v19\n"
    "v_and_b32 v20, s78, v20\n"
    "v_and_b32 v21, s78, v21\n"
    "v_and_b32 v22, s78, v22\n"
    "v_and_b32 v23, s78, v23\n"
    "v_and_b32 v24, s78, v24\n"
    "v_and_b32 v25, s78, v25\n"
    "v_and_b32 v26, s78, v26\n"
    "v_and_b32 v27, s78, v27\n"
    "v_and_b32 v28, s78, v28\n"
    "v_and_b32 v29, s78, v29\n"
    "v_and_b32 v30, s78, v30\n"
    "v_and_b32 v31, s78, v31\n"
    "v_pk_add_f32 v[16:17], v[16:17], v[20:21]\n"
    "v_pk_add_f32 v[18:19], v[18:19], v[22:23]\n"
    "v_pk_add_f32 v[24:25], v[24:25], v[28:29]\n"
    "v_pk_add_f32 v[26:27], v[26:27], v[30:31]\n"
    "v_pk_add_f32 v[16:17], v[16:17], v[24:25]\n"
    "v_pk_add_f32 v[18:19], v[18:19], v[26:27]\n"
    "s_nop 1\n"
    "v_permlane32_swap_b32 v16, v18\n"
    "v_permlane32_swap_b32 v17, v19\n"
    "v_pk_add_f32 v[16:17], v[16:17], v[18:19]\n"
    "s_nop 1\n"
    "v_permlane16_swap_b32 v16, v17\n"
    "v_add_f32 v16, v16, v17\n"
    "s_nop 1\n"
    "v_add_f32_dpp v16, v16, v16 row_ror:8 row_mask:0xf bank_mask:0xf bound_ctrl:1\n"
    "ds_bpermute_b32 v16, v11, v16\n"
    "s_waitcnt lgkmcnt(0)\n"
    "v_cvt_pk_bf16_f32 v16, v16, v16\n"
    "v_and_b32 v16, s62, v16\n"
    "v_mul_f32 v17, 0xbfb8aa3b, v16\n"
    "v_exp_f32 v17, v17\n"
    "s_nop 0\n"
    "v_add_f32 v17, 1.0, v17\n"
    "v_rcp_f32 v17, v17\n"
    "s_nop 0\n"
    "v_mul_f32 v21, v16, v17\n"
    "v_cvt_pk_bf16_f32 v21, v21, v21\n"
    "v_and_b32 v21, s62, v21\n"
    "v_lshlrev_b32 v14, 16, v14\n"
    "v_mov_b32 v10, s79\n"
    "s_mov_b32 s66, 0x11111111\n"
    "s_mov_b32 s67, 0x11111111\n"
    "s_cmp_lg_u32 s2, 0\n"
    "s_cbranch_scc1 .Lm_ne\n"
    "s_mov_b64 exec, 1\n"
    "buffer_store_dword v10, off, s[8:11], 0 sc1\n"
    "s_mov_b64 exec, -1\n"
    ".Lm_ne:\n"
    ".Lw_37:\n"
    "ds_read_b32 v8, v4 offset:44\n"
    "s_waitcnt lgkmcnt(0)\n"
    "v_readfirstlane_b32 s56, v8\n"
    "s_cmp_ge_u32 s56, 1\n"
    "s_cbranch_scc1 .Ld_38\n"
    "s_sleep 1\n"
    "s_sub_u32 s49, s49, 1\n"
    "s_cbranch_scc0 .Lw_37\n"
    "s_mov_b32 s49, 0\n"
    ".Ld_38:\n"
    ".Lm_l:\n"
    "ds_read_b32 v8, v4 offset:48\n"
    "ds_read_b128 v[16:19], v15 offset:4608\n"
    "s_waitcnt lgkmcnt(0)\n"
    "v_readfirstlane_b32 s56, v8\n"
    "v_pk_add_f32 v[16:17], v[16:17], v[18:19]\n"
    "v_add_f32 v16, v16, v17\n"
    "s_cmp_ge_u32 s56, 8\n"
    "s_cbranch_scc1 .Lm_ok\n"
    "s_sub_u32 s49, s49, 1\n"
    "s_cbranch_scc0 .Lm_l\n"
    "s_mov_b32 s49, 0\n"
    ".Lm_ok:\n"
    "v_add_f32_dpp v16, v16, v16 quad_perm:[1,0,3,2] row_mask:0xf bank_mask:0xf bound_ctrl:1\n"
    "v_cvt_pk_bf16_f32 v16, v16, v16\n"
    "v_and_b32 v16, s62, v16\n"
    "v_mul_f32 v16, v16, v21\n"
    "v_cvt_pk_bf16_f32 v16, v16, v16\n"
    "v_and_b32 v16, s62, v16\n"
    "v_add_f32 v16, v14, v16\n"
    "s_nop 1\n"
    "v_mov_b32_dpp v17, v16 quad_perm:[2,3,2,3] row_mask:0xf bank_mask:0xf bound_ctrl:1\n"
    "s_mov_b64 exec, s[66:67]\n"
    "v_cvt_pk_bf16_f32 v16, v16, v17\n"
    "buffer_store_dword v16, v12, s[36:39], 0 offen sc1\n"
    "s_endpgm\n"
    ".Lend:\n"
    ".p2align 12\n"
    :: "s"(__builtin_amdgcn_kernarg_segment_ptr()), "s"(__builtin_amdgcn_workgroup_id_x()), "v"(__builtin_amdgcn_workitem_id_x()),
       "s"((u32)(size_t)(__attribute__((address_space(3))) u32*)lds)
    : "v127", "s79", "vcc", "memory");
}

extern "C" void kernel_launch(void* const* d_in, const int* in_sizes, int n_in, void* d_out, int out_size, void* d_ws, size_t ws_size, hipStream_t stream) {
  const u32 F = 0x00020000u;
  const TileTab32 tab = {{0x460046, 0x47006a, 0x47, 0x560056, 0x570057, 0x66006b, 0x66, 0x670067, 0x314a2f4a, 0x31cb2fcb, 0x304c, 0x324c006c, 0x32cd30cd, 0x344e314e, 0x31cf, 0x34cf006d, 0x3550006e, 0x35d13250, 0x365232d1, 0x36d3006f, 0x37540000, 0x37d53452, 0x5834d3, 0x590000, 0x2d402a40, 0x2dc12ac1, 0x2e422c42, 0x2ec32cc3, 0x2f442d44, 0x2fc52dc5, 0x30482e48, 0x30c92ec9}};
  fused_kernel<<<256, 1024, 0, stream>>>(d_in[0], 1280u, F, d_ws, 839680u, F, d_in[8], 1966080u, F, d_in[3], 7864320u, F, d_in[4], 7864320u, F,
                                         d_in[6], 1966080u, F, d_in[7], 1966080u, F, d_in[1], 100663296u, F, d_out, 1280u, F, d_in[2], 100663296u, F, tab);
}
```
